```python
import torch

def _softmax(x, axis=-1):
    return torch.softmax(x, dim=axis)
def _gelu(x):
    return torch.nn.functional.gelu(x, approximate="tanh")
def _silu(x):
    return torch.nn.functional.silu(x)
def _sigmoid(x):
    return torch.sigmoid(x)
def _layernorm(x, axis=-1, eps=1e-5):
    m = x.mean(dim=axis, keepdim=True)
    v = x.var(dim=axis, keepdim=True, unbiased=False)
    return (x - m) / torch.sqrt(v + eps)
def _scatter_add(tgt, idx, src, axis=0):
    return tgt.clone().index_add_(axis, idx, src)
def _segment_sum(src, seg, n):
    out = torch.zeros((n,) + src.shape[1:], dtype=src.dtype)
    return out.index_add_(0, seg, src)
def _pad(x, pw):
    flat = tuple(v for pair in reversed(pw) for v in pair)
    return torch.nn.functional.pad(x, flat)

kernel_name = 'gated_mix__linear_attn_block__dense_activation_mish__prompt'

def setup_inputs():

    _w = (lambda a: (lambda t: t.to(torch.bfloat16) if t.is_floating_point() else t)(torch.as_tensor(a)))
    def _A_setup():
        B, S, D = 1, 2048, 1024
        return {
            "x": _w((lambda *s: torch.randn(*s, dtype=torch.bfloat16) * 0.05)(B, S, D)),
            "wqkv": _w((lambda *s: torch.randn(*s, dtype=torch.bfloat16) * 0.8)(D, 3 * D)),
            "wo": _w((lambda *s: torch.randn(*s, dtype=torch.bfloat16) * 0.05)(D, D)),
        }
    _A_in = _A_setup()

    def _B_setup():
        B, S, D, F = 1, 2048, 1024, 3584
        return {
            "x": _w((lambda *s: torch.randn(*s, dtype=torch.bfloat16) * 0.05)(B, S, D)),
            "w1": _w((lambda *s: torch.randn(*s, dtype=torch.bfloat16) * 12.8)(D, F) * (D ** -0.5)),
            "b1": _w((lambda *s: torch.randn(*s, dtype=torch.bfloat16) * 0.4)(F)),
            "w2": _w((lambda *s: torch.randn(*s, dtype=torch.bfloat16) * 0.05)(F, D) * (F ** -0.5)),
        }
    _B_in = _B_setup()

    return {
        "A_x": _A_in["x"],
        "A_wqkv": _A_in["wqkv"],
        "A_wo": _A_in["wo"],
        "B_x": _B_in["x"],
        "B_w1": _B_in["w1"],
        "B_b1": _B_in["b1"],
        "B_w2": _B_in["w2"],
        "gate_w": _w((lambda *s: torch.randn(*s, dtype=torch.bfloat16) * 0.05)(1024) * 16.0),
    }

def reference(A_x, A_wqkv, A_wo, B_x, B_w1, B_b1, B_w2, gate_w):
    def _A_ref(x, wqkv, wo):
        B, S, D = x.shape
        H = 32; Dh = D // H
        qkv = (lambda x, *d: x.permute(*d))((x @ wqkv).reshape(B, S, 3, H, Dh), 2, 0, 3, 1, 4)
        q, k, v = qkv[0], qkv[1], qkv[2]
        phi = lambda t: torch.where(t > 0, t + 1, torch.exp(t)) + 1e-6
        pq, pk = phi(q), phi(k)
        kv = torch.einsum("bhsd,bhse->bhde", pk, v)
        z = torch.einsum("bhsd->bhd", pk)
        num = torch.einsum("bhqd,bhde->bhqe", pq, kv)
        den = torch.einsum("bhqd,bhd->bhq", pq, z)[..., None] + 1e-6
        o = num / den
        return (lambda x, *d: x.permute(*d))(o, 0, 2, 1, 3).reshape(B, S, D) @ wo

    def _B_ref(x, w1, b1, w2):
        h = torch.einsum("bsd,df->bsf", x, w1) + b1
        sp = torch.log1p(torch.exp(-torch.abs(h))) + torch.where(h > 0, h, torch.zeros_like(h))
        act = h * torch.tanh(sp)
        return torch.einsum("bsf,fd->bsd", act, w2)

    x = A_x
    g = _sigmoid((x * gate_w).sum(-1, keepdims=True))
    a = _A_ref(x, A_wqkv, A_wo)
    b = _B_ref(x, B_w1, B_b1, B_w2)
    return g * a + (1 - g) * b
```

```cpp
#include <hip/hip_runtime.h>
#include <stdint.h>
#include <utility>
#define ALT 3
#define LAS __attribute__((address_space(3)))
#define DI __device__ __forceinline__
typedef unsigned short u16;
typedef short bf16x8 __attribute__((ext_vector_type(8)));
typedef short s16x4 __attribute__((ext_vector_type(4)));
typedef float f32x4 __attribute__((ext_vector_type(4)));
typedef float f32x2 __attribute__((ext_vector_type(2)));
typedef unsigned u32x4 __attribute__((ext_vector_type(4)));
typedef unsigned u32x2 __attribute__((ext_vector_type(2)));
typedef __amdgpu_buffer_rsrc_t rsrc_t;

constexpr int XA_LD = 4608;
constexpr size_t WS_XA = 0;
constexpr size_t WS_PQ = WS_XA + (size_t)2048 * XA_LD * 2;
constexpr size_t WS_KV = WS_PQ + (size_t)2048 * 1024 * 2;
constexpr size_t WS_Z  = WS_KV + (size_t)8 * 32 * 1024 * 4;
constexpr size_t WS_G  = WS_Z + (size_t)8 * 32 * 32 * 4;
constexpr size_t WS_FL = WS_G + 2048 * 4;
constexpr int N_FLAG_WORDS = 64 * 137;
constexpr size_t WS_PART = WS_FL + 65536;

#define SCHED() __builtin_amdgcn_sched_barrier(0)
#define WAIT_V(n) asm volatile("s_waitcnt vmcnt(%0)" ::"n"(n) : "memory")
#define WAIT_L(n) asm volatile("s_waitcnt lgkmcnt(%0)" ::"n"(n) : "memory")
#define BAR() __builtin_amdgcn_s_barrier()

template <int OFF> DI void rd128(bf16x8& d, unsigned a) { asm volatile("ds_read_b128 %0, %1 offset:%2" : "=v"(d) : "v"(a), "i"(OFF) : "memory"); }
template <int OFF> DI void rdtr(s16x4& d, unsigned a) { asm volatile("ds_read_b64_tr_b16 %0, %1 offset:%2" : "=v"(d) : "v"(a), "i"(OFF) : "memory"); }
DI void gload2(u32x2& d, const void* p) { asm volatile("global_load_dwordx2 %0, %1, off" : "=v"(d) : "v"(p) : "memory"); }
template <class T> DI void pin(T& v) { asm volatile("" : "+v"(v)); }
template <class T> DI void keep(const T& v) { asm volatile("" :: "v"(v)); }
DI rsrc_t mk_rsrc(const void* p, unsigned n = 0x7fffffff) { return __builtin_amdgcn_make_buffer_rsrc((void*)p, 0, n, 0x00020000); }
DI void dma16(rsrc_t r, unsigned voff, unsigned soff, LAS char* l) { __builtin_amdgcn_raw_ptr_buffer_load_lds(r, (LAS void*)l, 16, voff, soff, 0, 0); }
DI unsigned cvt_pk(float lo, float hi) { unsigned r; asm("v_cvt_pk_bf16_f32 %0, %1, %2" : "=v"(r) : "v"(lo), "v"(hi)); return r; }
DI float bflo(unsigned w) { return __uint_as_float(w << 16); }
DI float bfhi(unsigned w) { return __uint_as_float(w & 0xffff0000u); }
constexpr float LOG2E = 1.44269504088896f;
DI float phi_f(float t) { const float e = __builtin_amdgcn_exp2f(__builtin_fminf(t * LOG2E, 0.f)); return e + __builtin_fmaxf(t + 1e-6f, 1e-6f); }
DI f32x2 mish2(f32x2 h) {
  const f32x2 t = h * LOG2E;
  f32x2 e; e.x = __builtin_amdgcn_exp2f(t.x); e.y = __builtin_amdgcn_exp2f(t.y);
  const f32x2 a = __builtin_elementwise_fma(e, (f32x2){0.5f, 0.5f}, (f32x2){1.f, 1.f});
  const f32x2 d = __builtin_elementwise_fma(e, a, (f32x2){1.f, 1.f});
  f32x2 r; r.x = __builtin_amdgcn_rcpf(d.x); r.y = __builtin_amdgcn_rcpf(d.y);
  return __builtin_elementwise_fma(-h, r, h);
}
DI f32x4 mish4(const f32x4& v) { const f32x2 a = mish2((f32x2){v[0], v[1]}), b = mish2((f32x2){v[2], v[3]}); return (f32x4){a.x, a.y, b.x, b.y}; }
DI f32x4 phi4(const f32x4& v) { return (f32x4){phi_f(v[0]), phi_f(v[1]), phi_f(v[2]), phi_f(v[3])}; }
#define MFMA16(a, b, c) __builtin_amdgcn_mfma_f32_16x16x32_bf16((a), (b), (c), 0, 0, 0)
#define CAT(L, H) __builtin_shufflevector((L), (H), 0, 1, 2, 3, 4, 5, 6, 7)
template <class F, int... I> DI void static_for_impl(F&& f, std::integer_sequence<int, I...>) { (f(std::integral_constant<int, I>{}), ...); }
template <int N, class F> DI void static_for(F&& f) { static_for_impl(f, std::make_integer_sequence<int, N>{}); }

constexpr int K1_XS = 32768, K1_WS = 26624, K1_WOFF = 3 * K1_XS, K1_LDS = K1_WOFF + 2 * K1_WS;

struct K1Args {
  const u16* x; const u16* wqkv; const u16* w1; const u16* b1; const u16* gw; char* ws;
};

template <bool KV, int WN> constexpr int blk_panel(int b) { return b == 6 ? 3 : (KV ? (b >> 1) : (b < 4 ? WN : 2)); }
template <bool KV, int WN> constexpr int blk_cb(int b) { return b == 6 ? 0 : (KV ? 2 * WN + (b & 1) : (b < 4 ? b : 2 * WN + (b & 1))); }
template <bool KV, int WN> constexpr bool blk_std(int b) { return KV && b < 4; }
template <bool KV, int WN> constexpr bool blk_bias(int b) { return KV ? b >= 4 : (WN == 1 || b >= 4); }

template <bool KV, int WN>
DI void k1_body(const K1Args& A, LAS char* lds, int panel, int hp, int j32, int wm, int lane) {
  constexpr int NB = 7;
  const int wid = WN * 4 + wm;
#define STAMP_AT(i)
  const int G = hp >> 1, a = hp & 1;
  const int g4 = lane >> 4, l15 = lane & 15;
  const int c0 = (KV ? 1024 : 0) + 64 * hp;
  const int c1 = KV ? (2048 + 64 * hp) : (448 * G + 128 + 128 * a);
  const int c2 = KV ? (448 * G + 64 * a) : (448 * G + 128 + 128 * a + 64);
  const int c3 = 448 * G + 384 + (KV ? 0 : 32) + 16 * a;
  auto pcol = [&](int P) { return P == 0 ? c0 : P == 1 ? c1 : P == 2 ? c2 : c3; };
  u32x2 bia[NB];
  static_for<NB>([&](auto B) {
    constexpr int b = B;
    if constexpr (blk_bias<KV, WN>(b)) {
      const char* p = (const char*)A.b1 + (pcol(blk_panel<KV, WN>(b)) + 16 * blk_cb<KV, WN>(b) + 4 * g4) * 2;
      gload2(bia[b], p);
    }
  });
  rsrc_t rx = mk_rsrc(A.x), rq = mk_rsrc(A.wqkv), r1 = mk_rsrc(A.w1);
  unsigned voffX[2], voffWa, voffWb, voffN;
#pragma unroll
  for (int par = 0; par < 2; ++par)
    voffX[par] = (unsigned)((32 * wid + 8 * par + (lane >> 3)) * 2048 + (((lane & 7) ^ ((4 * par + (lane >> 4)) & 7)) * 16));
  {
    const int sw = ((lane >> 4) & 1) | ((wm & 1) << 1);
    const unsigned ch = (unsigned)(((lane & 7) ^ (sw << 1)) * 16);
    voffWa = (unsigned)((8 * wid + (lane >> 3)) * 6144) + ch;
    voffWb = (unsigned)((8 * wid + (lane >> 3)) * 7168) + ch;
    const int rho = 32 * (wm & 1) + (lane >> 1);
    voffN = (unsigned)((rho ^ (((rho >> 3) & 1) << 2)) * 7168 + (lane & 1) * 16);
  }
  const unsigned xrow0 = (unsigned)panel * 256 * 2048;
  auto issueX = [&](int t, int xs, int j) {
    dma16(rx, voffX[j & 1], xrow0 + t * 128 + (j >> 1) * 32768, lds + xs * K1_XS + (4 * wid + j) * 1024);
  };
  auto issueW = [&](int t, int P) {
    LAS char* dst = lds + K1_WOFF + (t & 1) * K1_WS + P * 8192 + wid * 1024;
    if (P == 0) dma16(rq, voffWa, t * (64 * 6144) + c0 * 2, dst);
    else if (P == 1) { if (KV) dma16(rq, voffWa, t * (64 * 6144) + c1 * 2, dst); else dma16(r1, voffWb, t * (64 * 7168) + c1 * 2, dst); }
    else if (P == 2) dma16(r1, voffWb, t * (64 * 7168) + c2 * 2, dst);
    else if (WN == 0 && wm < 2) dma16(r1, voffN, t * (64 * 7168) + c3 * 2, dst);
  };
#pragma unroll
  for (int j = 0; j < 4; ++j) issueX(0, 0, j);
#pragma unroll
  for (int j = 0; j < 4; ++j) issueW(0, j);
#pragma unroll
  for (int j = 0; j < 4; ++j) issueX(1, 1, j);
#pragma unroll
  for (int j = 0; j < 4; ++j) issueW(1, j);
  const unsigned ldsb = (unsigned)(size_t)lds;
  unsigned ax[2];
#pragma unroll
  for (int ks = 0; ks < 2; ++ks) ax[ks] = ldsb + (64 * wm + l15) * 128 + (((4 * ks + g4) ^ ((lane >> 1) & 7)) * 16);
  const int q4 = l15 >> 2, p4 = lane & 3;
  const int sr = ((q4 >> 1) & 1) | ((g4 & 1) << 1);
  unsigned aw[4];
#pragma unroll
  for (int c = 0; c < 4; ++c) aw[c] = ldsb + K1_WOFF + (8 * g4 + q4) * 128 + ((c ^ sr) * 32) + p4 * 8;
  unsigned an[2];
#pragma unroll
  for (int t = 0; t < 2; ++t) an[t] = ldsb + K1_WOFF + 24576 + (8 * g4 + ((4 * t) ^ (4 * (g4 & 1))) + q4) * 32 + p4 * 8;

  STAMP_AT(1);
  WAIT_V(7);
  f32x4 acc[4][NB];
  static_for<NB>([&](auto B) {
    constexpr int b = B;
    f32x4 init = {0.f, 0.f, 0.f, 0.f};
    if constexpr (blk_bias<KV, WN>(b)) {
      pin(bia[b]);
      const u32x2 w = bia[b]; init = (f32x4){bflo(w.x), bfhi(w.x), bflo(w.y), bfhi(w.y)};
    }
#pragma unroll
    for (int mb = 0; mb < 4; ++mb) acc[mb][b] = init;
  });
  BAR();
  STAMP_AT(2);

  bf16x8 Af[2][4]; s16x4 Bl[NB], Bh[NB];
  auto readA = [&](auto BUF, auto KS, auto MB, unsigned xo) { rd128<MB * 2048>(Af[BUF][MB], ax[KS] + xo); };
  auto readB = [&](auto KS, auto B, unsigned wo) {
    constexpr int b = B, P = blk_panel<KV, WN>(b), c = blk_cb<KV, WN>(b);
    if constexpr (P < 3) { rdtr<P * 8192 + KS * 4096>(Bl[b], aw[c] + wo); rdtr<P * 8192 + KS * 4096 + 512>(Bh[b], aw[c] + wo); }
    else { rdtr<KS * 1024>(Bl[b], an[0] + wo); rdtr<KS * 1024>(Bh[b], an[1] + wo); }
  };
  auto mma = [&](auto BUF, auto B, auto MB) {
    constexpr int b = B, mb = (b == 6) ? 2 * WN + MB : MB;
    if constexpr (blk_std<KV, WN>(b)) acc[mb][b] = MFMA16(Af[BUF][mb], CAT(Bl[b], Bh[b]), acc[mb][b]);
    else acc[mb][b] = MFMA16(CAT(Bl[b], Bh[b]), Af[BUF][mb], acc[mb][b]);
  };
  using I0 = std::integral_constant<int, 0>; using I1 = std::integral_constant<int, 1>;
  using I2 = std::integral_constant<int, 2>; using I3 = std::integral_constant<int, 3>;
  auto readBh = [&](auto KS, auto B, auto H, unsigned wo) {
    constexpr int b = B, P = blk_panel<KV, WN>(b), c = blk_cb<KV, WN>(b);
    if constexpr (P < 3) { if constexpr (H == 0) rdtr<P * 8192 + KS * 4096>(Bl[b], aw[c] + wo); else rdtr<P * 8192 + KS * 4096 + 512>(Bh[b], aw[c] + wo); }
    else { if constexpr (H == 0) rdtr<KS * 1024>(Bl[b], an[0] + wo); else rdtr<KS * 1024>(Bh[b], an[1] + wo); }
  };
  static_for<4>([&](auto MB) { readA(I0{}, I0{}, MB, 0u); });
  static_for<NB - 1>([&](auto B) { readB(I0{}, B, 0u); });
  constexpr int CAP = 15;
  constexpr int W0 = 2 * (NB - 2), W1 = (2 * NB - 2) > CAP ? CAP : (2 * NB - 2), W2 = (2 * NB) > CAP ? CAP : (2 * NB);
  if constexpr (WN == 0) __builtin_amdgcn_s_setprio(2); else __builtin_amdgcn_s_setprio(1);
  int xs = 0;
  for (int t = 0; t < 16; ++t) {
    const unsigned xo = xs * K1_XS, wo = (t & 1) * K1_WS;
    const int xs1 = xs == 2 ? 0 : xs + 1, xs2 = xs1 == 2 ? 0 : xs1 + 1;
    const unsigned xo1 = xs1 * K1_XS, wo1 = ((t + 1) & 1) * K1_WS;
    constexpr bool more = true;
    { const unsigned nrec = t < 14 ? 0x7fffffffu : 0u; rx = mk_rsrc(A.x, nrec); rq = mk_rsrc(A.wqkv, nrec); r1 = mk_rsrc(A.w1, nrec); }
    static_for<NB>([&](auto B) {
      constexpr int b = B;
      if constexpr (b == 0) WAIT_L(W0); else if constexpr (b == 1) WAIT_L(W1); else WAIT_L(W2);
      if constexpr (WN == 0 && b == ALT) __builtin_amdgcn_s_setprio(0);
      SCHED();
      static_for<(b == 6 ? 2 : 4)>([&](auto MB) {
        constexpr int s = b * 4 + MB;
        mma(I0{}, B, MB);
        if constexpr (s < 2) readBh(I0{}, std::integral_constant<int, NB - 1>{}, MB, wo);
        if constexpr (s == 2) readA(I1{}, I1{}, I0{}, xo);
        if constexpr (s == 3) readA(I1{}, I1{}, I1{}, xo);
        if constexpr (s == 6) readA(I1{}, I1{}, I2{}, xo);
        if constexpr (s == 7) readA(I1{}, I1{}, I3{}, xo);
        if constexpr (b >= 1 && MB < 2) readBh(I1{}, std::integral_constant<int, (b >= 1 ? b - 1 : 0)>{}, MB, wo);
        if constexpr (more) { if constexpr (s >= 2 && (s - 2) % 6 == 0 && s < 24) issueX(t + 2, xs2, (s - 2) / 6); }
        SCHED();
      });
    });
    static_for<NB>([&](auto B) {
      constexpr int b = B;
      if constexpr (b <= 1) WAIT_L(W0);
      if constexpr (b == 2) {
        WAIT_L(0);
        WAIT_V(4);
        BAR();
        if constexpr (WN == 0) __builtin_amdgcn_s_setprio(2);
      }
      SCHED();
      static_for<(b == 6 ? 2 : 4)>([&](auto MB) {
        constexpr int s = b * 4 + MB;
        mma(I1{}, B, MB);
        if constexpr (s < 2) readBh(I1{}, std::integral_constant<int, NB - 1>{}, MB, wo);
        if constexpr (s == 8) { readA(I0{}, I0{}, I0{}, xo1); readA(I0{}, I0{}, I1{}, xo1); }
        if constexpr (s == 9) { readA(I0{}, I0{}, I2{}, xo1); readA(I0{}, I0{}, I3{}, xo1); }
        if constexpr (s == 10) readB(I0{}, I0{}, wo1);
        if constexpr (s == 11) readB(I0{}, I1{}, wo1);
        if constexpr (b >= 3 && MB < 2) readBh(I0{}, std::integral_constant<int, (b >= 3 ? b - 1 : 0)>{}, MB, wo1);
        if constexpr (more) { if constexpr (s >= 9 && (s - 9) % 4 == 0 && s < 25) issueW(t + 2, (s - 9) / 4); }
        SCHED();
      });
    });
    xs = xs1;
  }
  WAIT_V(0);
  BAR();
  WAIT_L(0); SCHED();
  __builtin_amdgcn_s_setprio(0);
  STAMP_AT(3);

  u16* xa = (u16*)(A.ws + WS_XA);
  const size_t rowbase = (size_t)panel * 256 + 64 * wm;
  LAS char* stg = lds + (WN * 4 + wm) * 8192;
  const int chunk_g = (g4 & 1) * 2 + (g4 >> 1);
  const u32x4* xr = (const u32x4*)((const char*)A.x + (size_t)(panel * 256 + j32 * 8 + WN * 4 + wm) * 2048 + lane * 32);
  const u32x4* gr = (const u32x4*)((const char*)A.gw + lane * 32);
  const u32x4 gx0 = xr[0], gx1 = xr[1], gg0 = gr[0], gg1 = gr[1];
  auto store_line = [&](auto B0, auto ACT, u16* base, size_t ld, int col0) {
    static_for<4>([&](auto MB) {
      constexpr int mb = MB;
      static_for<2>([&](auto PP) {
        constexpr int p = PP;
        f32x4 e = acc[mb][B0 + 2 * p], o = acc[mb][B0 + 2 * p + 1];
        if constexpr (ACT == 0) { e = phi4(e); o = phi4(o); } else { e = mish4(e); o = mish4(o); }
        const unsigned e0 = cvt_pk(e[0], e[1]), e1 = cvt_pk(e[2], e[3]), o0 = cvt_pk(o[0], o[1]), o1 = cvt_pk(o[2], o[3]);
        const auto r0 = __builtin_amdgcn_permlane16_swap(e0, o0, false, false);
        const auto r1 = __builtin_amdgcn_permlane16_swap(e1, o1, false, false);
        const u32x4 w = {r0[0], r1[0], r0[1], r1[1]};
        *(LAS u32x4*)(stg + (16 * mb + l15) * 128 + (((4 * p + chunk_g) ^ (l15 & 7)) * 16)) = w;
      });
    });
#pragma unroll
    for (int i = 0; i < 8; ++i) {
      const u32x4 w = *(LAS u32x4*)(stg + i * 1024 + lane * 16);
      const int r = 8 * i + (lane >> 3), ch = (lane & 7) ^ (r & 7);
      *(u32x4*)(base + (rowbase + r) * ld + col0 + ch * 8) = w;
    }
  };
  auto store_half = [&](int col0) {
#pragma unroll
    for (int mb = 0; mb < 4; ++mb) {
      const f32x4 e = mish4(acc[mb][4]), o = mish4(acc[mb][5]);
      const unsigned e0 = cvt_pk(e[0], e[1]), e1 = cvt_pk(e[2], e[3]), o0 = cvt_pk(o[0], o[1]), o1 = cvt_pk(o[2], o[3]);
      const auto r0 = __builtin_amdgcn_permlane16_swap(e0, o0, false, false);
      const auto r1 = __builtin_amdgcn_permlane16_swap(e1, o1, false, false);
      const u32x4 w = {r0[0], r1[0], r0[1], r1[1]};
      *(u32x4*)(xa + (rowbase + 16 * mb + l15) * XA_LD + col0 + chunk_g * 8) = w;
    }
  };
  auto store_narrow = [&]() {
#pragma unroll
    for (int m2 = 0; m2 < 2; ++m2) {
      const int mb = 2 * WN + m2;
      const f32x4 v = mish4(acc[2 * WN + m2][6]);
      const u32x2 w = {cvt_pk(v[0], v[1]), cvt_pk(v[2], v[3])};
      *(u32x2*)(xa + (rowbase + 16 * mb + l15) * XA_LD + c3 + 4 * g4) = w;
    }
  };
  store_half(c2 + 32 * WN);
  store_narrow();
  if constexpr (!KV) {
    if constexpr (WN == 0) store_line(I0{}, I0{}, (u16*)(A.ws + WS_PQ), 1024, c0);
    else store_line(I0{}, I1{}, xa, XA_LD, c1);
  } else {
    f32x4 kvD[4]; f32x4 zD[2];
#pragma unroll
    for (int i = 0; i < 4; ++i) kvD[i] = (f32x4){0.f, 0.f, 0.f, 0.f};
    zD[0] = zD[1] = (f32x4){0.f, 0.f, 0.f, 0.f};
    const bf16x8 ones = {0x3f80, 0x3f80, 0x3f80, 0x3f80, 0x3f80, 0x3f80, 0x3f80, 0x3f80};
#pragma unroll
    for (int mp = 0; mp < 2; ++mp) {
      bf16x8 ka[2], vb[2];
#pragma unroll
      for (int b = 0; b < 2; ++b) {
        const f32x4 k0 = phi4(acc[2 * mp][b]), k1 = phi4(acc[2 * mp + 1][b]);
        const u32x4 w = {cvt_pk(k0[0], k0[1]), cvt_pk(k0[2], k0[3]), cvt_pk(k1[0], k1[1]), cvt_pk(k1[2], k1[3])};
        ka[b] = __builtin_bit_cast(bf16x8, w);
        const f32x4 v0 = acc[2 * mp][2 + b], v1 = acc[2 * mp + 1][2 + b];
        const u32x4 u = {cvt_pk(v0[0], v0[1]), cvt_pk(v0[2], v0[3]), cvt_pk(v1[0], v1[1]), cvt_pk(v1[2], v1[3])};
        vb[b] = __builtin_bit_cast(bf16x8, u);
      }
#pragma unroll
      for (int db = 0; db < 2; ++db) {
#pragma unroll
        for (int eb = 0; eb < 2; ++eb) kvD[db * 2 + eb] = MFMA16(ka[db], vb[eb], kvD[db * 2 + eb]);
        zD[db] = MFMA16(ka[db], ones, zD[db]);
      }
    }
    LAS char* sc = lds + 2 * K1_XS;
    LAS char* sz = lds + K1_WOFF;
#pragma unroll
    for (int i = 0; i < 4; ++i) *(LAS f32x4*)(sc + ((WN * 4 + wm) * 4 + i) * 1024 + lane * 16) = kvD[i];
    if (l15 == 0) {
#pragma unroll
      for (int db = 0; db < 2; ++db) *(LAS f32x4*)(sz + ((WN * 4 + wm) * 2 + db) * 64 + g4 * 16) = zD[db];
    }
    WAIT_L(0); BAR(); SCHED();
    const int blk = wm;
    f32x4 s = {0.f, 0.f, 0.f, 0.f};
#pragma unroll
    for (int m = 0; m < 4; ++m) s += *(LAS f32x4*)(sc + ((WN * 4 + m) * 4 + blk) * 1024 + lane * 16);
    float* kvp = (float*)(A.ws + WS_KV) + ((size_t)panel * 32 + 2 * hp + WN) * 1024;
    *(f32x4*)(kvp + (16 * (blk & 1) + l15) * 32 + 16 * (blk >> 1) + 4 * g4) = s;
    if (wm == 0 && lane < 8) {
      const int db = lane >> 2, gq = lane & 3;
      f32x4 zs = {0.f, 0.f, 0.f, 0.f};
#pragma unroll
      for (int m = 0; m < 4; ++m) zs += *(LAS f32x4*)(sz + ((WN * 4 + m) * 2 + db) * 64 + gq * 16);
      *(f32x4*)((float*)(A.ws + WS_Z) + ((size_t)panel * 32 + 2 * hp + WN) * 32 + 16 * db + 4 * gq) = zs;
    }
  }
  {
    float s = 0.f;
#pragma unroll
    for (int i = 0; i < 4; ++i) {
      s = __builtin_fmaf(bflo(gx0[i]), bflo(gg0[i]), s); s = __builtin_fmaf(bfhi(gx0[i]), bfhi(gg0[i]), s);
      s = __builtin_fmaf(bflo(gx1[i]), bflo(gg1[i]), s); s = __builtin_fmaf(bfhi(gx1[i]), bfhi(gg1[i]), s);
    }
#pragma unroll
    for (int o = 32; o >= 1; o >>= 1) s += __shfl_xor(s, o);
    const float gval = __builtin_amdgcn_rcpf(1.f + __builtin_amdgcn_exp2f(-LOG2E * s));
    if (lane == 0) ((float*)(A.ws + WS_G))[panel * 256 + j32 * 8 + WN * 4 + wm] = gval;
  }
}

__global__ __launch_bounds__(512, 2) void k1_kernel(K1Args A) {
  extern __shared__ __attribute__((aligned(16))) char smem[];
  LAS char* lds = (LAS char*)smem;
  const int b = blockIdx.x, xcd = b & 7, idx = b >> 3, rq = xcd >> 1, ch = xcd & 1;
  const int panel = 2 * rq + (idx >> 4), c16 = idx & 15, hp = 8 * ch + (c16 >> 1), j32 = ch * 16 + c16;
  const int wid = __builtin_amdgcn_readfirstlane(threadIdx.x >> 6), lane = threadIdx.x & 63;
  if (b == 0 && threadIdx.x < 137) ((unsigned*)(A.ws + WS_FL))[threadIdx.x * 64] = 0u;
  if (c16 & 1) {
    if (wid < 4) k1_body<true, 0>(A, lds, panel, hp, j32, wid, lane); else k1_body<true, 1>(A, lds, panel, hp, j32, wid - 4, lane);
  } else {
    if (wid < 4) k1_body<false, 0>(A, lds, panel, hp, j32, wid, lane); else k1_body<false, 1>(A, lds, panel, hp, j32, wid - 4, lane);
  }
}

#define K2_NA 28
constexpr int K2_ST = 32768, K2_NST = 4, K2_LDS = K2_NST * K2_ST + 16384;
struct K2Args { const u16* w2; const u16* wo; char* ws; u16* out; };
typedef __attribute__((address_space(1))) unsigned gu32;
#define RLX_AGENT __ATOMIC_RELAXED, __HIP_MEMORY_SCOPE_AGENT
DI void dma16_sc1(rsrc_t r, unsigned voff, unsigned soff, LAS char* l) { __builtin_amdgcn_raw_ptr_buffer_load_lds(r, (LAS void*)l, 16, voff, soff, 0, 16); }
DI bool wait_ge(gu32* word, unsigned want, gu32* tmo) {
  for (unsigned spins = 0;; ++spins) {
    if ((unsigned)__builtin_amdgcn_readfirstlane(__hip_atomic_load(word, RLX_AGENT)) >= want) return true;
    if (spins > (1u << 20)) { __hip_atomic_store(tmo, 1u, RLX_AGENT); return false; }
    __builtin_amdgcn_s_sleep(2);
  }
}

DI void k2_prep(const K2Args& A, LAS char* lds, int p, int hp, int wid, int lane) {
  const int tid = wid * 64 + lane, g4 = lane >> 4, l15 = lane & 15;
  LAS char* kvs = lds + K2_NST * K2_ST;
  const float* kvp = (const float*)(A.ws + WS_KV) + (size_t)(2 * hp) * 1024 + tid * 4;
  f32x4 s = {0.f, 0.f, 0.f, 0.f};
#pragma unroll
  for (int q = 0; q < 8; ++q) s += *(const f32x4*)(kvp + (size_t)q * 32 * 1024);
  const size_t row0 = (size_t)p * 256 + 32 * wid;
  const u16* pq = (const u16*)(A.ws + WS_PQ);
  bf16x8 pf[2][2]; float gv[2];
#pragma unroll
  for (int mb = 0; mb < 2; ++mb) {
#pragma unroll
    for (int h = 0; h < 2; ++h) pf[mb][h] = *(const bf16x8*)(pq + (row0 + 16 * mb + l15) * 1024 + 64 * hp + 32 * h + 8 * g4);
    gv[mb] = ((const float*)(A.ws + WS_G))[row0 + 16 * mb + l15];
  }
  *(LAS u32x2*)(kvs + tid * 8) = (u32x2){cvt_pk(s[0], s[1]), cvt_pk(s[2], s[3])};
  if (tid < 16) {
    const float* zp = (const float*)(A.ws + WS_Z) + (size_t)(2 * hp) * 32 + tid * 4;
    f32x4 z = {0.f, 0.f, 0.f, 0.f};
#pragma unroll
    for (int q = 0; q < 8; ++q) z += *(const f32x4*)(zp + (size_t)q * 32 * 32);
    *(LAS u32x2*)(kvs + 4096 + tid * 8) = (u32x2){cvt_pk(z[0], z[1]), cvt_pk(z[2], z[3])};
  }
  __syncthreads();
  const rsrc_t ro = mk_rsrc(A.ws + WS_XA);
  const int chunk_g = (g4 & 1) * 2 + (g4 >> 1);
#pragma unroll
  for (int h = 0; h < 2; ++h) {
    bf16x8 ka[2];
#pragma unroll
    for (int eb = 0; eb < 2; ++eb) ka[eb] = *(LAS bf16x8*)(kvs + h * 2048 + (16 * eb + l15) * 64 + g4 * 16);
    const bf16x8 za = *(LAS bf16x8*)(kvs + 4096 + h * 64 + g4 * 16);
#pragma unroll
    for (int mb = 0; mb < 2; ++mb) {
      const f32x4 zero = {0.f, 0.f, 0.f, 0.f};
      const f32x4 n0 = MFMA16(ka[0], pf[mb][h], zero), n1 = MFMA16(ka[1], pf[mb][h], zero), dn = MFMA16(za, pf[mb][h], zero);
      const float sc = gv[mb] * __builtin_amdgcn_rcpf(dn[0] + 1e-6f);
      const f32x4 e = n0 * sc, o = n1 * sc;
      const unsigned e0 = cvt_pk(e[0], e[1]), e1 = cvt_pk(e[2], e[3]), o0 = cvt_pk(o[0], o[1]), o1 = cvt_pk(o[2], o[3]);
      const auto r0 = __builtin_amdgcn_permlane16_swap(e0, o0, false, false);
      const auto r1 = __builtin_amdgcn_permlane16_swap(e1, o1, false, false);
      const u32x4 w = {r0[0], r1[0], r0[1], r1[1]};
      const unsigned off = (unsigned)(((row0 + 16 * mb + l15) * XA_LD + 3584 + 64 * hp + 32 * h + chunk_g * 8) * 2);
      __builtin_amdgcn_raw_buffer_store_b128(w, ro, off, 0, 16);
    }
  }
  WAIT_V(0);
  __syncthreads();
  if (tid == 0) __hip_atomic_fetch_add((gu32*)(A.ws + WS_FL) + 64 * p, 1u, RLX_AGENT);
}

__global__ __launch_bounds__(512, 2) void k2_kernel(K2Args A) {
  extern __shared__ __attribute__((aligned(16))) char smem[];
  LAS char* lds = (LAS char*)smem;
  const int b = blockIdx.x, xcd = b & 7, idx = b >> 3, rq = xcd >> 1, role = xcd & 1;
  const int tm = 4 * rq + (idx >> 3), tn = idx & 7;
  const int wid = __builtin_amdgcn_readfirstlane(threadIdx.x >> 6), lane = threadIdx.x & 63;
  const int kh = wid >> 2, wm = (wid >> 1) & 1, wn = wid & 1;
  const int g4 = lane >> 4, l15 = lane & 15;
  gu32* flags = (gu32*)(A.ws + WS_FL);
  const int nt = role == 0 ? K2_NA + 16 : 56 - K2_NA;
  auto ktile = [&](int i) { return role == 0 ? (i < K2_NA ? i : i + (56 - K2_NA)) : K2_NA + i; };
  unsigned voffX[2];
#pragma unroll
  for (int par = 0; par < 2; ++par)
    voffX[par] = (unsigned)((16 * wid + 8 * par + (lane >> 3)) * (XA_LD * 2) + (((lane & 7) ^ ((4 * par + (lane >> 4)) & 7)) * 16));
  const int sw = ((lane >> 4) & 1) | ((wid & 1) << 1);
  const unsigned voffW = (unsigned)((8 * wid + (lane >> 3)) * 2048 + (((lane & 7) ^ (sw << 1)) * 16));
  const unsigned xrow0 = (unsigned)tm * 128 * (XA_LD * 2);
  auto issue = [&](int i, int j) {
    const bool live = i < nt;
    const int kt = ktile(i);
    LAS char* st = lds + (i & 3) * K2_ST;
    const unsigned nrec = live ? 0x7fffffffu : 0u;
    if (j < 2) dma16_sc1(mk_rsrc(A.ws + WS_XA, nrec), voffX[j], xrow0 + kt * 128, st + (2 * wid + j) * 1024);
    else dma16(mk_rsrc(kt < 56 ? A.w2 : A.wo, nrec), voffW, (kt < 56 ? kt : kt - 56) * (64 * 2048) + tn * 256 + (j - 2) * 128, st + 16384 + (j - 2) * 8192 + wid * 1024);
  };
  if (role == 1) {
#pragma unroll
    for (int i = 0; i < 3; ++i)
#pragma unroll
      for (int j = 0; j < 4; ++j) issue(i, j);
    k2_prep(A, lds, rq * 2 + (idx >> 4), idx & 15, wid, lane);
  } else {
#pragma unroll
    for (int i = 0; i < 3; ++i)
#pragma unroll
      for (int j = 0; j < 4; ++j) issue(i, j);
  }
  float omg[4];
#pragma unroll
  for (int mb = 0; mb < 4; ++mb) omg[mb] = 1.f - ((const float*)(A.ws + WS_G))[tm * 128 + 64 * wm + 16 * mb + l15];
#pragma unroll
  for (int mb = 0; mb < 4; ++mb) pin(omg[mb]);
  const unsigned ldsb = (unsigned)(size_t)lds;
  const unsigned ax = ldsb + (64 * wm + l15) * 128 + (((4 * kh + g4) ^ ((lane >> 1) & 7)) * 16);
  const int q4 = l15 >> 2, p4 = lane & 3;
  const int sr = ((q4 >> 1) & 1) | ((g4 & 1) << 1);
  unsigned aw[4];
#pragma unroll
  for (int c = 0; c < 4; ++c) aw[c] = ldsb + 16384 + wn * 8192 + kh * 4096 + (8 * g4 + q4) * 128 + ((c ^ sr) * 32) + p4 * 8;
  f32x4 acc[4][4];
#pragma unroll
  for (int i = 0; i < 4; ++i)
#pragma unroll
    for (int j = 0; j < 4; ++j) acc[i][j] = (f32x4){0.f, 0.f, 0.f, 0.f};
  bf16x8 Af[2][4]; s16x4 Bl[2][4], Bh[2][4];
  auto readAll = [&](auto BUF, auto I, unsigned so) {
    constexpr int i = I;
    if constexpr (i < 4) rd128<i * 2048>(Af[BUF][i], ax + so);
    else if constexpr (((i - 4) & 1) == 0) rdtr<0>(Bl[BUF][(i - 4) >> 1], aw[(i - 4) >> 1] + so);
    else rdtr<512>(Bh[BUF][(i - 4) >> 1], aw[(i - 4) >> 1] + so);
  };
  using I0 = std::integral_constant<int, 0>; using I1 = std::integral_constant<int, 1>;
  issue(3, 0); issue(3, 1); issue(3, 2); issue(3, 3);
  WAIT_V(12); BAR(); SCHED();
  static_for<12>([&](auto I) { readAll(I0{}, I, 0u); });
  auto body = [&](auto CUR, int i) {
    if (role == 0 && i == K2_NA - 4 && wid == 0) wait_ge(flags + 64 * (tm >> 1), 16u, flags + 64 * 136);
    WAIT_L(0); WAIT_V(8); BAR(); SCHED();
    if (role == 0 && i == K2_NA) {
#pragma unroll
      for (int mb = 0; mb < 4; ++mb)
#pragma unroll
        for (int nb = 0; nb < 4; ++nb) acc[mb][nb] *= omg[mb];
    }
    const unsigned so1 = ((i + 1) & 3) * K2_ST;
    static_for<16>([&](auto S) {
      constexpr int s = S, nb = s >> 2, mb = s & 3;
      acc[mb][nb] = MFMA16(CAT(Bl[CUR][nb], Bh[CUR][nb]), Af[CUR][mb], acc[mb][nb]);
      if constexpr (s < 12) readAll(std::integral_constant<int, CUR ^ 1>{}, S, so1);
      if constexpr ((s & 3) == 1) issue(i + 4, s >> 2);
      SCHED();
    });
  };
  for (int i = 0; i < nt; i += 2) { body(I0{}, i); body(I1{}, i + 1); }
  WAIT_L(0); WAIT_V(0); BAR(); SCHED();
  if (role == 1) {
#pragma unroll
    for (int mb = 0; mb < 4; ++mb)
#pragma unroll
      for (int nb = 0; nb < 4; ++nb) acc[mb][nb] *= omg[mb];
  }
  {
    LAS char* ex = lds + (wid & 3) * 16384;
#pragma unroll
    for (int m2 = 0; m2 < 2; ++m2)
#pragma unroll
      for (int nb = 0; nb < 4; ++nb) {
        const f32x4 v = kh == 0 ? acc[2 + m2][nb] : acc[m2][nb];
        *(LAS f32x4*)(ex + kh * 8192 + (m2 * 4 + nb) * 1024 + lane * 16) = v;
      }
    __syncthreads();
  }
  f32x4 fin[2][4];
  {
    LAS char* ex = lds + (wid & 3) * 16384;
#pragma unroll
    for (int m2 = 0; m2 < 2; ++m2)
#pragma unroll
      for (int nb = 0; nb < 4; ++nb) {
        const f32x4 o = *(LAS f32x4*)(ex + (kh ^ 1) * 8192 + (m2 * 4 + nb) * 1024 + lane * 16);
        fin[m2][nb] = (kh == 0 ? acc[m2][nb] : acc[2 + m2][nb]) + o;
      }
  }
  const int tile = tm * 8 + tn;
  const rsrc_t rp = mk_rsrc(A.ws + WS_PART);
  const unsigned poff = (unsigned)((tile * 8 + wid) * 8192 + lane * 16);
  if (role == 1) {
#pragma unroll
    for (int m2 = 0; m2 < 2; ++m2)
#pragma unroll
      for (int nb = 0; nb < 4; ++nb) __builtin_amdgcn_raw_buffer_store_b128(__builtin_bit_cast(u32x4, fin[m2][nb]), rp, poff + (m2 * 4 + nb) * 1024, 0, 16);
    WAIT_V(0);
    __syncthreads();
    if (threadIdx.x == 0) __hip_atomic_store(flags + 64 * (8 + tile), 1u, RLX_AGENT);
  } else {
    if (wid == 0) wait_ge(flags + 64 * (8 + tile), 1u, flags + 64 * 136);
    __syncthreads();
#pragma unroll
    for (int m2 = 0; m2 < 2; ++m2)
#pragma unroll
      for (int nb = 0; nb < 4; ++nb) fin[m2][nb] += __builtin_bit_cast(f32x4, __builtin_amdgcn_raw_buffer_load_b128(rp, poff + (m2 * 4 + nb) * 1024, 0, 16));
    const int chunk_g = (g4 & 1) * 2 + (g4 >> 1);
#pragma unroll
    for (int m2 = 0; m2 < 2; ++m2)
#pragma unroll
      for (int pp = 0; pp < 2; ++pp) {
        const f32x4 e = fin[m2][2 * pp], o = fin[m2][2 * pp + 1];
        const unsigned e0 = cvt_pk(e[0], e[1]), e1 = cvt_pk(e[2], e[3]), o0 = cvt_pk(o[0], o[1]), o1 = cvt_pk(o[2], o[3]);
        const auto r0 = __builtin_amdgcn_permlane16_swap(e0, o0, false, false);
        const auto r1 = __builtin_amdgcn_permlane16_swap(e1, o1, false, false);
        const u32x4 w = {r0[0], r1[0], r0[1], r1[1]};
        *(u32x4*)(A.out + (size_t)(tm * 128 + 64 * wm + 32 * kh + 16 * m2 + l15) * 1024 + tn * 128 + 64 * wn + 32 * pp + chunk_g * 8) = w;
      }
  }
}

extern "C" void kernel_launch(void* const* d_in, const int* in_sizes, int n_in, void* d_out, int out_size, void* d_ws, size_t ws_size, hipStream_t stream) {
  K1Args a; a.x = (const u16*)d_in[0]; a.wqkv = (const u16*)d_in[1]; a.w1 = (const u16*)d_in[4]; a.b1 = (const u16*)d_in[5]; a.gw = (const u16*)d_in[7]; a.ws = (char*)d_ws;
  (void)hipFuncSetAttribute((const void*)k1_kernel, hipFuncAttributeMaxDynamicSharedMemorySize, K1_LDS);
  (void)hipFuncSetAttribute((const void*)k2_kernel, hipFuncAttributeMaxDynamicSharedMemorySize, K2_LDS);
  k1_kernel<<<256, 512, K1_LDS, stream>>>(a);
  K2Args c; c.w2 = (const u16*)d_in[6]; c.wo = (const u16*)d_in[2]; c.ws = (char*)d_ws; c.out = (u16*)d_out;
  k2_kernel<<<256, 512, K2_LDS, stream>>>(c);
}
```

```cpp
#include <hip/hip_runtime.h>
#include <stdint.h>
#include <utility>
#define ALT 3
#define LAS __attribute__((address_space(3)))
#define DI __device__ __forceinline__
typedef unsigned short u16;
typedef short bf16x8 __attribute__((ext_vector_type(8)));
typedef short s16x4 __attribute__((ext_vector_type(4)));
typedef float f32x4 __attribute__((ext_vector_type(4)));
typedef float f32x2 __attribute__((ext_vector_type(2)));
typedef unsigned u32x4 __attribute__((ext_vector_type(4)));
typedef unsigned u32x2 __attribute__((ext_vector_type(2)));
typedef __amdgpu_buffer_rsrc_t rsrc_t;

constexpr int XA_LD = 4608;
constexpr size_t WS_XA = 0;
constexpr size_t WS_PQ = WS_XA + (size_t)2048 * XA_LD * 2;
constexpr size_t WS_KV = WS_PQ + (size_t)2048 * 1024 * 2;
constexpr size_t WS_Z  = WS_KV + (size_t)8 * 32 * 1024 * 4;
constexpr size_t WS_G  = WS_Z + (size_t)8 * 32 * 32 * 4;
constexpr size_t WS_FL = WS_G + 2048 * 4;
constexpr int N_FLAG_WORDS = 64 * 137;
constexpr size_t WS_PART = WS_FL + 65536;

#define SCHED() __builtin_amdgcn_sched_barrier(0)
#define WAIT_V(n) asm volatile("s_waitcnt vmcnt(%0)" ::"n"(n) : "memory")
#define WAIT_L(n) asm volatile("s_waitcnt lgkmcnt(%0)" ::"n"(n) : "memory")
#define BAR() __builtin_amdgcn_s_barrier()

template <int OFF> DI void rd128(bf16x8& d, unsigned a) { asm volatile("ds_read_b128 %0, %1 offset:%2" : "=v"(d) : "v"(a), "i"(OFF) : "memory"); }
template <int OFF> DI void rdtr(s16x4& d, unsigned a) { asm volatile("ds_read_b64_tr_b16 %0, %1 offset:%2" : "=v"(d) : "v"(a), "i"(OFF) : "memory"); }
DI void gload1(float& d, const void* p) { asm volatile("global_load_dword %0, %1, off" : "=v"(d) : "v"(p) : "memory"); }
DI void gload2(u32x2& d, const void* p) { asm volatile("global_load_dwordx2 %0, %1, off" : "=v"(d) : "v"(p) : "memory"); }
template <class T> DI void pin(T& v) { asm volatile("" : "+v"(v)); }
template <class T> DI void keep(const T& v) { asm volatile("" :: "v"(v)); }
DI rsrc_t mk_rsrc(const void* p, unsigned n = 0x7fffffff) { return __builtin_amdgcn_make_buffer_rsrc((void*)p, 0, n, 0x00020000); }
DI void dma16(rsrc_t r, unsigned voff, unsigned soff, LAS char* l) { __builtin_amdgcn_raw_ptr_buffer_load_lds(r, (LAS void*)l, 16, voff, soff, 0, 0); }
DI unsigned cvt_pk(float lo, float hi) { unsigned r; asm("v_cvt_pk_bf16_f32 %0, %1, %2" : "=v"(r) : "v"(lo), "v"(hi)); return r; }
DI float bflo(unsigned w) { return __uint_as_float(w << 16); }
DI float bfhi(unsigned w) { return __uint_as_float(w & 0xffff0000u); }
constexpr float LOG2E = 1.44269504088896f;
DI float phi_f(float t) { const float e = __builtin_amdgcn_exp2f(__builtin_fminf(t * LOG2E, 0.f)); return e + __builtin_fmaxf(t + 1e-6f, 1e-6f); }
DI f32x2 mish2(f32x2 h) {
  const f32x2 t = h * LOG2E;
  f32x2 e; e.x = __builtin_amdgcn_exp2f(t.x); e.y = __builtin_amdgcn_exp2f(t.y);
  const f32x2 a = __builtin_elementwise_fma(e, (f32x2){0.5f, 0.5f}, (f32x2){1.f, 1.f});
  const f32x2 d = __builtin_elementwise_fma(e, a, (f32x2){1.f, 1.f});
  f32x2 r; r.x = __builtin_amdgcn_rcpf(d.x); r.y = __builtin_amdgcn_rcpf(d.y);
  return __builtin_elementwise_fma(-h, r, h);
}
DI f32x4 mish4(const f32x4& v) { const f32x2 a = mish2((f32x2){v[0], v[1]}), b = mish2((f32x2){v[2], v[3]}); return (f32x4){a.x, a.y, b.x, b.y}; }
DI f32x4 phi4(const f32x4& v) { return (f32x4){phi_f(v[0]), phi_f(v[1]), phi_f(v[2]), phi_f(v[3])}; }
#define MFMA16(a, b, c) __builtin_amdgcn_mfma_f32_16x16x32_bf16((a), (b), (c), 0, 0, 0)
#define CAT(L, H) __builtin_shufflevector((L), (H), 0, 1, 2, 3, 4, 5, 6, 7)
template <class F, int... I> DI void static_for_impl(F&& f, std::integer_sequence<int, I...>) { (f(std::integral_constant<int, I>{}), ...); }
template <int N, class F> DI void static_for(F&& f) { static_for_impl(f, std::make_integer_sequence<int, N>{}); }

constexpr int K1_XS = 32768, K1_WS = 26624, K1_WOFF = 3 * K1_XS, K1_DUMMY = K1_WOFF + 2 * K1_WS, K1_LDS = K1_DUMMY + 1024;

struct K1Args {
  const u16* x; const u16* wqkv; const u16* w1; const u16* b1; const u16* gw; char* ws; const u16* w2; const u16* wo;
};
DI void touch(const void* p, LAS char* dummy) { __builtin_amdgcn_global_load_lds((const unsigned*)p, (LAS unsigned*)dummy, 4, 0, 0); }

template <bool KV, int WN> constexpr int blk_panel(int b) { return b == 6 ? 3 : (KV ? (b >> 1) : (b < 4 ? WN : 2)); }
template <bool KV, int WN> constexpr int blk_cb(int b) { return b == 6 ? 0 : (KV ? 2 * WN + (b & 1) : (b < 4 ? b : 2 * WN + (b & 1))); }
template <bool KV, int WN> constexpr bool blk_std(int b) { return KV && b < 4; }
template <bool KV, int WN> constexpr bool blk_bias(int b) { return KV ? b >= 4 : (WN == 1 || b >= 4); }

template <bool KV, int WN>
DI void k1_body(const K1Args& A, LAS char* lds, int panel, int hp, int j32, int wm, int lane) {
  constexpr int NB = 7;
  const int wid = WN * 4 + wm;
#define STAMP_AT(i)
  const int G = hp >> 1, a = hp & 1;
  const int g4 = lane >> 4, l15 = lane & 15;
  const int c0 = (KV ? 1024 : 0) + 64 * hp;
  const int c1 = KV ? (2048 + 64 * hp) : (448 * G + 128 + 128 * a);
  const int c2 = KV ? (448 * G + 64 * a) : (448 * G + 128 + 128 * a + 64);
  const int c3 = 448 * G + 384 + (KV ? 0 : 32) + 16 * a;
  auto pcol = [&](int P) { return P == 0 ? c0 : P == 1 ? c1 : P == 2 ? c2 : c3; };
  u32x2 bia[NB];
  static_for<NB>([&](auto B) {
    constexpr int b = B;
    if constexpr (blk_bias<KV, WN>(b)) {
      const char* p = (const char*)A.b1 + (pcol(blk_panel<KV, WN>(b)) + 16 * blk_cb<KV, WN>(b) + 4 * g4) * 2;
      gload2(bia[b], p);
    }
  });
  rsrc_t rx = mk_rsrc(A.x), rq = mk_rsrc(A.wqkv), r1 = mk_rsrc(A.w1);
  unsigned voffX[2], voffWa, voffWb, voffN;
#pragma unroll
  for (int par = 0; par < 2; ++par)
    voffX[par] = (unsigned)((32 * wid + 8 * par + (lane >> 3)) * 2048 + (((lane & 7) ^ ((4 * par + (lane >> 4)) & 7)) * 16));
  {
    const int sw = ((lane >> 4) & 1) | ((wm & 1) << 1);
    const unsigned ch = (unsigned)(((lane & 7) ^ (sw << 1)) * 16);
    voffWa = (unsigned)((8 * wid + (lane >> 3)) * 6144) + ch;
    voffWb = (unsigned)((8 * wid + (lane >> 3)) * 7168) + ch;
    const int rho = 32 * (wm & 1) + (lane >> 1);
    voffN = (unsigned)((rho ^ (((rho >> 3) & 1) << 2)) * 7168 + (lane & 1) * 16);
  }
  const unsigned xrow0 = (unsigned)panel * 256 * 2048;
  auto issueX = [&](int t, int xs, int j) {
    dma16(rx, voffX[j & 1], xrow0 + t * 128 + (j >> 1) * 32768, lds + xs * K1_XS + (4 * wid + j) * 1024);
  };
  auto issueW = [&](int t, int P) {
    LAS char* dst = lds + K1_WOFF + (t & 1) * K1_WS + P * 8192 + wid * 1024;
    if (P == 0) dma16(rq, voffWa, t * (64 * 6144) + c0 * 2, dst);
    else if (P == 1) { if (KV) dma16(rq, voffWa, t * (64 * 6144) + c1 * 2, dst); else dma16(r1, voffWb, t * (64 * 7168) + c1 * 2, dst); }
    else if (P == 2) dma16(r1, voffWb, t * (64 * 7168) + c2 * 2, dst);
    else if (WN == 0 && wm < 2) dma16(r1, voffN, t * (64 * 7168) + c3 * 2, dst);
  };
#pragma unroll
  for (int j = 0; j < 4; ++j) issueX(0, 0, j);
#pragma unroll
  for (int j = 0; j < 4; ++j) issueW(0, j);
#pragma unroll
  for (int j = 0; j < 4; ++j) issueX(1, 1, j);
#pragma unroll
  for (int j = 0; j < 4; ++j) issueW(1, j);
  const unsigned ldsb = (unsigned)(size_t)lds;
  unsigned ax[2];
#pragma unroll
  for (int ks = 0; ks < 2; ++ks) ax[ks] = ldsb + (64 * wm + l15) * 128 + (((4 * ks + g4) ^ ((lane >> 1) & 7)) * 16);
  const int q4 = l15 >> 2, p4 = lane & 3;
  const int sr = ((q4 >> 1) & 1) | ((g4 & 1) << 1);
  unsigned aw[4];
#pragma unroll
  for (int c = 0; c < 4; ++c) aw[c] = ldsb + K1_WOFF + (8 * g4 + q4) * 128 + ((c ^ sr) * 32) + p4 * 8;
  unsigned an[2];
#pragma unroll
  for (int t = 0; t < 2; ++t) an[t] = ldsb + K1_WOFF + 24576 + (8 * g4 + ((4 * t) ^ (4 * (g4 & 1))) + q4) * 32 + p4 * 8;

  STAMP_AT(1);
  WAIT_V(7);
  f32x4 acc[4][NB];
  static_for<NB>([&](auto B) {
    constexpr int b = B;
    f32x4 init = {0.f, 0.f, 0.f, 0.f};
    if constexpr (blk_bias<KV, WN>(b)) {
      pin(bia[b]);
      const u32x2 w = bia[b]; init = (f32x4){bflo(w.x), bfhi(w.x), bflo(w.y), bfhi(w.y)};
    }
#pragma unroll
    for (int mb = 0; mb < 4; ++mb) acc[mb][b] = init;
  });
  BAR();
  STAMP_AT(2);

  bf16x8 Af[2][4]; s16x4 Bl[NB], Bh[NB];
  auto readA = [&](auto BUF, auto KS, auto MB, unsigned xo) { rd128<MB * 2048>(Af[BUF][MB], ax[KS] + xo); };
  auto readB = [&](auto KS, auto B, unsigned wo) {
    constexpr int b = B, P = blk_panel<KV, WN>(b), c = blk_cb<KV, WN>(b);
    if constexpr (P < 3) { rdtr<P * 8192 + KS * 4096>(Bl[b], aw[c] + wo); rdtr<P * 8192 + KS * 4096 + 512>(Bh[b], aw[c] + wo); }
    else { rdtr<KS * 1024>(Bl[b], an[0] + wo); rdtr<KS * 1024>(Bh[b], an[1] + wo); }
  };
  auto mma = [&](auto BUF, auto B, auto MB) {
    constexpr int b = B, mb = (b == 6) ? 2 * WN + MB : MB;
    if constexpr (blk_std<KV, WN>(b)) acc[mb][b] = MFMA16(Af[BUF][mb], CAT(Bl[b], Bh[b]), acc[mb][b]);
    else acc[mb][b] = MFMA16(CAT(Bl[b], Bh[b]), Af[BUF][mb], acc[mb][b]);
  };
  using I0 = std::integral_constant<int, 0>; using I1 = std::integral_constant<int, 1>;
  using I2 = std::integral_constant<int, 2>; using I3 = std::integral_constant<int, 3>;
  auto readBh = [&](auto KS, auto B, auto H, unsigned wo) {
    constexpr int b = B, P = blk_panel<KV, WN>(b), c = blk_cb<KV, WN>(b);
    if constexpr (P < 3) { if constexpr (H == 0) rdtr<P * 8192 + KS * 4096>(Bl[b], aw[c] + wo); else rdtr<P * 8192 + KS * 4096 + 512>(Bh[b], aw[c] + wo); }
    else { if constexpr (H == 0) rdtr<KS * 1024>(Bl[b], an[0] + wo); else rdtr<KS * 1024>(Bh[b], an[1] + wo); }
  };
  static_for<4>([&](auto MB) { readA(I0{}, I0{}, MB, 0u); });
  static_for<NB - 1>([&](auto B) { readB(I0{}, B, 0u); });
  constexpr int CAP = 15;
  constexpr int W0 = 2 * (NB - 2), W1 = (2 * NB - 2) > CAP ? CAP : (2 * NB - 2), W2 = (2 * NB) > CAP ? CAP : (2 * NB);
  if constexpr (WN == 0) __builtin_amdgcn_s_setprio(2); else __builtin_amdgcn_s_setprio(1);
  unsigned tdummy = 0;
  int xs = 0;
  auto ktile = [&](auto MORE, auto VMW, int t) {
    const unsigned xo = xs * K1_XS, wo = (t & 1) * K1_WS;
    const int xs1 = xs == 2 ? 0 : xs + 1, xs2 = xs1 == 2 ? 0 : xs1 + 1;
    const unsigned xo1 = xs1 * K1_XS, wo1 = ((t + 1) & 1) * K1_WS;
    constexpr bool more = decltype(MORE)::value;
    static_for<NB>([&](auto B) {
      constexpr int b = B;
      if constexpr (b == 0) WAIT_L(W0); else if constexpr (b == 1) WAIT_L(W1); else WAIT_L(W2);
      if constexpr (WN == 0 && b == ALT) __builtin_amdgcn_s_setprio(0);
      SCHED();
      static_for<(b == 6 ? 2 : 4)>([&](auto MB) {
        constexpr int s = b * 4 + MB;
        mma(I0{}, B, MB);
        if constexpr (s < 2) readBh(I0{}, std::integral_constant<int, NB - 1>{}, MB, wo);
        if constexpr (s == 2) readA(I1{}, I1{}, I0{}, xo);
        if constexpr (s == 3) readA(I1{}, I1{}, I1{}, xo);
        if constexpr (s == 6) readA(I1{}, I1{}, I2{}, xo);
        if constexpr (s == 7) readA(I1{}, I1{}, I3{}, xo);
        if constexpr (b >= 1 && MB < 2) readBh(I1{}, std::integral_constant<int, (b >= 1 ? b - 1 : 0)>{}, MB, wo);
        if constexpr (s >= 2 && (s - 2) % 6 == 0 && s < 24) { if constexpr (more) issueX(t + 2, xs2, (s - 2) / 6); }
        SCHED();
      });
    });
    static_for<NB>([&](auto B) {
      constexpr int b = B;
      if constexpr (b <= 1) WAIT_L(W0);
      if constexpr (b == 2) {
        WAIT_L(0);
        { constexpr int vmw = decltype(VMW)::value; if constexpr (vmw >= 0) WAIT_V(vmw >= 0 ? vmw : 0); }
        BAR();
        if constexpr (WN == 0) __builtin_amdgcn_s_setprio(2);
      }
      SCHED();
      static_for<(b == 6 ? 2 : 4)>([&](auto MB) {
        constexpr int s = b * 4 + MB;
        mma(I1{}, B, MB);
        if constexpr (s < 2) readBh(I1{}, std::integral_constant<int, NB - 1>{}, MB, wo);
        if constexpr (decltype(VMW)::value >= 0) {
          if constexpr (s == 8) { readA(I0{}, I0{}, I0{}, xo1); readA(I0{}, I0{}, I1{}, xo1); }
          if constexpr (s == 9) { readA(I0{}, I0{}, I2{}, xo1); readA(I0{}, I0{}, I3{}, xo1); }
          if constexpr (s == 10) readB(I0{}, I0{}, wo1);
          if constexpr (s == 11) readB(I0{}, I1{}, wo1);
          if constexpr (b >= 3 && MB < 2) readBh(I0{}, std::integral_constant<int, (b >= 3 ? b - 1 : 0)>{}, MB, wo1);
        }
        if constexpr (s >= 9 && (s - 9) % 4 == 0 && s < 25) { if constexpr (more) issueW(t + 2, (s - 9) / 4); }
        SCHED();
      });
    });
    xs = xs1;
  };
  for (int t = 0; t < 14; ++t) ktile(std::true_type{}, std::integral_constant<int, 4>{}, t);
  {
      const int li = (blockIdx.x * 8 + wid) * 36 + (lane < 36 ? lane : 0);
      const char* p = li < 57344 ? (const char*)A.w2 + (size_t)li * 128 : (const char*)A.wo + (size_t)(li - 57344) * 128;
      asm volatile("global_load_dword %0, %1, off" : "+v"(tdummy) : "v"(p) : "memory");
      SCHED();
    }
  ktile(std::false_type{}, std::integral_constant<int, 1>{}, 14);
  ktile(std::false_type{}, std::integral_constant<int, -1>{}, 15);
  WAIT_L(0); SCHED();
  __builtin_amdgcn_s_setprio(0);
  STAMP_AT(3);

  u16* xa = (u16*)(A.ws + WS_XA);
  const size_t rowbase = (size_t)panel * 256 + 64 * wm;
  LAS char* stg = lds + (WN * 4 + wm) * 8192;
  const int chunk_g = (g4 & 1) * 2 + (g4 >> 1);
  const u32x4* xr = (const u32x4*)((const char*)A.x + (size_t)(panel * 256 + j32 * 8 + WN * 4 + wm) * 2048 + lane * 32);
  const u32x4* gr = (const u32x4*)((const char*)A.gw + lane * 32);
  const u32x4 gx0 = xr[0], gx1 = xr[1], gg0 = gr[0], gg1 = gr[1];
  auto store_line = [&](auto B0, auto ACT, u16* base, size_t ld, int col0) {
    static_for<4>([&](auto MB) {
      constexpr int mb = MB;
      static_for<2>([&](auto PP) {
        constexpr int p = PP;
        f32x4 e = acc[mb][B0 + 2 * p], o = acc[mb][B0 + 2 * p + 1];
        if constexpr (ACT == 0) { e = phi4(e); o = phi4(o); } else { e = mish4(e); o = mish4(o); }
        const unsigned e0 = cvt_pk(e[0], e[1]), e1 = cvt_pk(e[2], e[3]), o0 = cvt_pk(o[0], o[1]), o1 = cvt_pk(o[2], o[3]);
        const auto r0 = __builtin_amdgcn_permlane16_swap(e0, o0, false, false);
        const auto r1 = __builtin_amdgcn_permlane16_swap(e1, o1, false, false);
        const u32x4 w = {r0[0], r1[0], r0[1], r1[1]};
        *(LAS u32x4*)(stg + (16 * mb + l15) * 128 + (((4 * p + chunk_g) ^ (l15 & 7)) * 16)) = w;
      });
    });
#pragma unroll
    for (int i = 0; i < 8; ++i) {
      const u32x4 w = *(LAS u32x4*)(stg + i * 1024 + lane * 16);
      const int r = 8 * i + (lane >> 3), ch = (lane & 7) ^ (r & 7);
      *(u32x4*)(base + (rowbase + r) * ld + col0 + ch * 8) = w;
    }
  };
  auto store_half = [&](int col0) {
#pragma unroll
    for (int mb = 0; mb < 4; ++mb) {
      const f32x4 e = mish4(acc[mb][4]), o = mish4(acc[mb][5]);
      const unsigned e0 = cvt_pk(e[0], e[1]), e1 = cvt_pk(e[2], e[3]), o0 = cvt_pk(o[0], o[1]), o1 = cvt_pk(o[2], o[3]);
      const auto r0 = __builtin_amdgcn_permlane16_swap(e0, o0, false, false);
      const auto r1 = __builtin_amdgcn_permlane16_swap(e1, o1, false, false);
      const u32x4 w = {r0[0], r1[0], r0[1], r1[1]};
      *(u32x4*)(xa + (rowbase + 16 * mb + l15) * XA_LD + col0 + chunk_g * 8) = w;
    }
  };
  auto store_narrow = [&]() {
#pragma unroll
    for (int m2 = 0; m2 < 2; ++m2) {
      const int mb = 2 * WN + m2;
      const f32x4 v = mish4(acc[2 * WN + m2][6]);
      const u32x2 w = {cvt_pk(v[0], v[1]), cvt_pk(v[2], v[3])};
      *(u32x2*)(xa + (rowbase + 16 * mb + l15) * XA_LD + c3 + 4 * g4) = w;
    }
  };
  store_half(c2 + 32 * WN);
  store_narrow();
  if constexpr (!KV) {
    if constexpr (WN == 0) store_line(I0{}, I0{}, (u16*)(A.ws + WS_PQ), 1024, c0);
    else store_line(I0{}, I1{}, xa, XA_LD, c1);
  } else {
    f32x4 kvD[4]; f32x4 zD[2];
#pragma unroll
    for (int i = 0; i < 4; ++i) kvD[i] = (f32x4){0.f, 0.f, 0.f, 0.f};
    zD[0] = zD[1] = (f32x4){0.f, 0.f, 0.f, 0.f};
    const bf16x8 ones = {0x3f80, 0x3f80, 0x3f80, 0x3f80, 0x3f80, 0x3f80, 0x3f80, 0x3f80};
#pragma unroll
    for (int mp = 0; mp < 2; ++mp) {
      bf16x8 ka[2], vb[2];
#pragma unroll
      for (int b = 0; b < 2; ++b) {
        const f32x4 k0 = phi4(acc[2 * mp][b]), k1 = phi4(acc[2 * mp + 1][b]);
        const u32x4 w = {cvt_pk(k0[0], k0[1]), cvt_pk(k0[2], k0[3]), cvt_pk(k1[0], k1[1]), cvt_pk(k1[2], k1[3])};
        ka[b] = __builtin_bit_cast(bf16x8, w);
        const f32x4 v0 = acc[2 * mp][2 + b], v1 = acc[2 * mp + 1][2 + b];
        const u32x4 u = {cvt_pk(v0[0], v0[1]), cvt_pk(v0[2], v0[3]), cvt_pk(v1[0], v1[1]), cvt_pk(v1[2], v1[3])};
        vb[b] = __builtin_bit_cast(bf16x8, u);
      }
#pragma unroll
      for (int db = 0; db < 2; ++db) {
#pragma unroll
        for (int eb = 0; eb < 2; ++eb) kvD[db * 2 + eb] = MFMA16(ka[db], vb[eb], kvD[db * 2 + eb]);
        zD[db] = MFMA16(ka[db], ones, zD[db]);
      }
    }
    LAS char* sc = lds + 2 * K1_XS;
    LAS char* sz = lds + K1_WOFF;
#pragma unroll
    for (int i = 0; i < 4; ++i) *(LAS f32x4*)(sc + ((WN * 4 + wm) * 4 + i) * 1024 + lane * 16) = kvD[i];
    if (l15 == 0) {
#pragma unroll
      for (int db = 0; db < 2; ++db) *(LAS f32x4*)(sz + ((WN * 4 + wm) * 2 + db) * 64 + g4 * 16) = zD[db];
    }
    WAIT_L(0); BAR(); SCHED();
    const int blk = wm;
    f32x4 s = {0.f, 0.f, 0.f, 0.f};
#pragma unroll
    for (int m = 0; m < 4; ++m) s += *(LAS f32x4*)(sc + ((WN * 4 + m) * 4 + blk) * 1024 + lane * 16);
    float* kvp = (float*)(A.ws + WS_KV) + ((size_t)panel * 32 + 2 * hp + WN) * 1024;
    *(f32x4*)(kvp + (16 * (blk & 1) + l15) * 32 + 16 * (blk >> 1) + 4 * g4) = s;
    if (wm == 0 && lane < 8) {
      const int db = lane >> 2, gq = lane & 3;
      f32x4 zs = {0.f, 0.f, 0.f, 0.f};
#pragma unroll
      for (int m = 0; m < 4; ++m) zs += *(LAS f32x4*)(sz + ((WN * 4 + m) * 2 + db) * 64 + gq * 16);
      *(f32x4*)((float*)(A.ws + WS_Z) + ((size_t)panel * 32 + 2 * hp + WN) * 32 + 16 * db + 4 * gq) = zs;
    }
  }
  {
    float s = 0.f;
#pragma unroll
    for (int i = 0; i < 4; ++i) {
      s = __builtin_fmaf(bflo(gx0[i]), bflo(gg0[i]), s); s = __builtin_fmaf(bfhi(gx0[i]), bfhi(gg0[i]), s);
      s = __builtin_fmaf(bflo(gx1[i]), bflo(gg1[i]), s); s = __builtin_fmaf(bfhi(gx1[i]), bfhi(gg1[i]), s);
    }
#pragma unroll
    for (int o = 32; o >= 1; o >>= 1) s += __shfl_xor(s, o);
    const float gval = __builtin_amdgcn_rcpf(1.f + __builtin_amdgcn_exp2f(-LOG2E * s));
    if (lane == 0) ((float*)(A.ws + WS_G))[panel * 256 + j32 * 8 + WN * 4 + wm] = gval;
  }
  WAIT_V(0); keep(tdummy);
}

__global__ __launch_bounds__(512, 2) void k1_kernel(K1Args A) {
  extern __shared__ __attribute__((aligned(16))) char smem[];
  LAS char* lds = (LAS char*)smem;
  const int b = blockIdx.x, xcd = b & 7, idx = b >> 3, rq = xcd >> 1, ch = xcd & 1;
  const int panel = 2 * rq + (idx >> 4), c16 = idx & 15, hp = 8 * ch + (c16 >> 1), j32 = ch * 16 + c16;
  const int wid = __builtin_amdgcn_readfirstlane(threadIdx.x >> 6), lane = threadIdx.x & 63;
  if (b == 0 && threadIdx.x < 137) ((unsigned*)(A.ws + WS_FL))[threadIdx.x * 64] = 0u;
  if (c16 & 1) {
    if (wid < 4) k1_body<true, 0>(A, lds, panel, hp, j32, wid, lane); else k1_body<true, 1>(A, lds, panel, hp, j32, wid - 4, lane);
  } else {
    if (wid < 4) k1_body<false, 0>(A, lds, panel, hp, j32, wid, lane); else k1_body<false, 1>(A, lds, panel, hp, j32, wid - 4, lane);
  }
}

#define K2_NA 22
#define K2_NST 5
constexpr int K2_ST = 32768, K2_SCR = 4 * K2_ST, K2_LDS = K2_NST == 5 ? 5 * K2_ST : 4 * K2_ST + 8192;
struct K2Args { const u16* w2; const u16* wo; char* ws; u16* out; };
typedef __attribute__((address_space(1))) unsigned gu32;
#define RLX_AGENT __ATOMIC_RELAXED, __HIP_MEMORY_SCOPE_AGENT
DI void dma16_sc1(rsrc_t r, unsigned voff, unsigned soff, LAS char* l) { __builtin_amdgcn_raw_ptr_buffer_load_lds(r, (LAS void*)l, 16, voff, soff, 0, 16); }
DI void gload4(u32x4& d, const void* p) { asm volatile("global_load_dwordx4 %0, %1, off" : "=v"(d) : "v"(p) : "memory"); }
DI bool wait_ge(gu32* word, unsigned want, gu32* tmo) {
  for (unsigned spins = 0;; ++spins) {
    if ((unsigned)__builtin_amdgcn_readfirstlane(__hip_atomic_load(word, RLX_AGENT)) >= want) return true;
    if (spins > (1u << 20)) { __hip_atomic_store(tmo, 1u, RLX_AGENT); return false; }
    __builtin_amdgcn_s_sleep(2);
  }
}

__global__ __launch_bounds__(576, 3) void k2_kernel(K2Args A) {
  extern __shared__ __attribute__((aligned(16))) char smem[];
  LAS char* lds = (LAS char*)smem;
  const int b = blockIdx.x, xcd = b & 7, idx = b >> 3, rq = xcd >> 1, role = xcd & 1;
  const int tm = 4 * rq + (idx >> 3), tn = idx & 7;
  const int wid = __builtin_amdgcn_readfirstlane(threadIdx.x >> 6), lane = threadIdx.x & 63;
  gu32* flags = (gu32*)(A.ws + WS_FL);
  const int tile = tm * 8 + tn;
  const int pp_ = 2 * rq + (idx >> 4), hh = 16 * role + (idx & 15);
  const int nt = role == 0 ? K2_NA + 16 : 56 - K2_NA;
  auto ktile = [&](int i) { return role == 0 ? (i < K2_NA ? i : i + (56 - K2_NA)) : K2_NA + i; };
  if (wid == 8) {
    BAR();
    BAR();
    for (int i = 0; i < nt; ++i) {
      if (role == 0 && i == K2_NA - K2_NST - 1) wait_ge(flags + 64 * (tm >> 1), 32u, flags + 64 * 136);
      BAR();
      if (i == 2 && lane == 0) __hip_atomic_fetch_add(flags + 64 * pp_, 1u, RLX_AGENT);
    }
    if (role == 0) wait_ge(flags + 64 * (8 + tile), 1u, flags + 64 * 136);
    BAR();
    BAR();
    if (role == 1) { BAR(); if (lane == 0) __hip_atomic_store(flags + 64 * (8 + tile), 1u, RLX_AGENT); }
    return;
  }
  const int kh = wid >> 2, wm = (wid >> 1) & 1, wn = wid & 1;
  const int g4 = lane >> 4, l15 = lane & 15;
  u32x2 part[8]; u32x4 zq, pf[2]; float gv[2], gq[4];
  const size_t prow = (size_t)pp_ * 256 + 32 * wid;
  {
    const float* kvp = (const float*)(A.ws + WS_KV) + (size_t)hh * 1024 + (wid * 64 + lane) * 2;
#pragma unroll
    for (int q = 0; q < 8; ++q) gload2(part[q], kvp + (size_t)q * 32 * 1024);
    gload4(zq, (const float*)(A.ws + WS_Z) + (size_t)(lane >> 3) * 1024 + hh * 32 + (lane & 7) * 4);
#pragma unroll
    for (int mb = 0; mb < 2; ++mb) {
      gload4(pf[mb], (const u16*)(A.ws + WS_PQ) + (prow + 16 * mb + l15) * 1024 + 32 * hh + 8 * g4);
      gload1(gv[mb], (const float*)(A.ws + WS_G) + prow + 16 * mb + l15);
    }
#pragma unroll
    for (int mb = 0; mb < 4; ++mb) gload1(gq[mb], (const float*)(A.ws + WS_G) + tm * 128 + 64 * wm + 16 * mb + l15);
  }
  unsigned voffX[2];
#pragma unroll
  for (int par = 0; par < 2; ++par)
    voffX[par] = (unsigned)((16 * wid + 8 * par + (lane >> 3)) * (XA_LD * 2) + (((lane & 7) ^ ((4 * par + (lane >> 4)) & 7)) * 16));
  const int sw = ((lane >> 4) & 1) | ((wid & 1) << 1);
  const unsigned voffW = (unsigned)((8 * wid + (lane >> 3)) * 2048 + (((lane & 7) ^ (sw << 1)) * 16));
  const unsigned xrow0 = (unsigned)tm * 128 * (XA_LD * 2);
  auto issue = [&](int i, int stage, int j) {
    const bool live = i < nt;
    const int kt = ktile(i);
    LAS char* st = lds + stage * K2_ST;
    const unsigned nrec = live ? 0x7fffffffu : 0u;
    if (j < 2) dma16_sc1(mk_rsrc(A.ws + WS_XA, nrec), voffX[j], xrow0 + kt * 128, st + (2 * wid + j) * 1024);
    else dma16(mk_rsrc(kt < 56 ? A.w2 : A.wo, nrec), voffW, (kt < 56 ? kt : kt - 56) * (64 * 2048) + tn * 256 + (j - 2) * 128, st + 16384 + (j - 2) * 8192 + wid * 1024);
  };
#pragma unroll
  for (int i = 0; i < 4; ++i)
#pragma unroll
    for (int j = 0; j < 4; ++j) issue(i, i, j);
  {
    WAIT_V(16);
#pragma unroll
    for (int q = 0; q < 8; ++q) pin(part[q]);
    pin(zq); pin(pf[0]); pin(pf[1]); pin(gv[0]); pin(gv[1]);
#pragma unroll
    for (int mb = 0; mb < 4; ++mb) pin(gq[mb]);
    LAS char* kvs = lds + K2_SCR;
    float s0 = 0.f, s1 = 0.f;
#pragma unroll
    for (int q = 0; q < 8; ++q) { s0 += __uint_as_float(part[q].x); s1 += __uint_as_float(part[q].y); }
    *(LAS unsigned*)(kvs + (wid * 64 + lane) * 4) = cvt_pk(s0, s1);
    if (wid == 0) {
      f32x4 z = __builtin_bit_cast(f32x4, zq);
#pragma unroll
      for (int o = 8; o <= 32; o <<= 1) { z[0] += __shfl_xor(z[0], o); z[1] += __shfl_xor(z[1], o); z[2] += __shfl_xor(z[2], o); z[3] += __shfl_xor(z[3], o); }
      if (lane < 8) *(LAS u32x2*)(kvs + 2048 + lane * 8) = (u32x2){cvt_pk(z[0], z[1]), cvt_pk(z[2], z[3])};
    }
    WAIT_L(0); BAR(); SCHED();
    bf16x8 ka[2], za;
    const unsigned kb = (unsigned)(size_t)kvs;
    rd128<0>(ka[0], kb + l15 * 64 + g4 * 16); rd128<1024>(ka[1], kb + l15 * 64 + g4 * 16); rd128<2048>(za, kb + g4 * 16);
    WAIT_L(0); SCHED();
    const rsrc_t ro = mk_rsrc(A.ws + WS_XA);
    const int chunk_g = (g4 & 1) * 2 + (g4 >> 1);
#pragma unroll
    for (int mb = 0; mb < 2; ++mb) {
      const f32x4 zero = {0.f, 0.f, 0.f, 0.f};
      const bf16x8 pb = __builtin_bit_cast(bf16x8, pf[mb]);
      const f32x4 n0 = MFMA16(ka[0], pb, zero), n1 = MFMA16(ka[1], pb, zero), dn = MFMA16(za, pb, zero);
      const float sc = gv[mb] * __builtin_amdgcn_rcpf(dn[0] + 1e-6f);
      const f32x4 e = n0 * sc, o = n1 * sc;
      const unsigned e0 = cvt_pk(e[0], e[1]), e1 = cvt_pk(e[2], e[3]), o0 = cvt_pk(o[0], o[1]), o1 = cvt_pk(o[2], o[3]);
      const auto r0 = __builtin_amdgcn_permlane16_swap(e0, o0, false, false);
      const auto r1 = __builtin_amdgcn_permlane16_swap(e1, o1, false, false);
      const u32x4 w = {r0[0], r1[0], r0[1], r1[1]};
      const unsigned off = (unsigned)(((prow + 16 * mb + l15) * XA_LD + 3584 + 32 * hh + chunk_g * 8) * 2);
      __builtin_amdgcn_raw_buffer_store_b128(w, ro, off, 0, 16);
    }
  }
  STAMP_AT(1);
  const unsigned ldsb = (unsigned)(size_t)lds;
  const unsigned ax = ldsb + (64 * wm + l15) * 128 + (((4 * kh + g4) ^ ((lane >> 1) & 7)) * 16);
  const int q4 = l15 >> 2, p4 = lane & 3;
  const int sr = ((q4 >> 1) & 1) | ((g4 & 1) << 1);
  unsigned aw[4];
#pragma unroll
  for (int c = 0; c < 4; ++c) aw[c] = ldsb + 16384 + wn * 8192 + kh * 4096 + (8 * g4 + q4) * 128 + ((c ^ sr) * 32) + p4 * 8;
  f32x4 acc[4][4];
#pragma unroll
  for (int i = 0; i < 4; ++i)
#pragma unroll
    for (int j = 0; j < 4; ++j) acc[i][j] = (f32x4){0.f, 0.f, 0.f, 0.f};
  bf16x8 Af[2][4]; s16x4 Bl[2][4], Bh[2][4];
  auto readAll = [&](auto BUF, auto I, unsigned so) {
    constexpr int i = I;
    if constexpr (i < 4) rd128<i * 2048>(Af[BUF][i], ax + so);
    else if constexpr (((i - 4) & 1) == 0) rdtr<0>(Bl[BUF][(i - 4) >> 1], aw[(i - 4) >> 1] + so);
    else rdtr<512>(Bh[BUF][(i - 4) >> 1], aw[(i - 4) >> 1] + so);
  };
  using I0 = std::integral_constant<int, 0>; using I1 = std::integral_constant<int, 1>;
  WAIT_V(14);
  BAR(); SCHED();
  STAMP_AT(2);
  float omg[4];
#pragma unroll
  for (int mb = 0; mb < 4; ++mb) omg[mb] = 1.f - gq[mb];
  static_for<12>([&](auto I) { readAll(I0{}, I, 0u); });
  int st = 0;
  auto body = [&](auto CUR, int i) {
    WAIT_L(0);
    if (K2_NST == 5 && i > 0) WAIT_V(12); else WAIT_V(8);
    BAR(); SCHED();
    if (role == 0 && i == K2_NA) {
#pragma unroll
      for (int mb = 0; mb < 4; ++mb)
#pragma unroll
        for (int nb = 0; nb < 4; ++nb) acc[mb][nb] *= omg[mb];
    }
    if (K2_NST == 5 && i == 0) { issue(4, 4, 0); issue(4, 4, 1); issue(4, 4, 2); issue(4, 4, 3); }
    const int st1 = st + 1 == K2_NST ? 0 : st + 1;
    const unsigned so1 = st1 * K2_ST;
    static_for<16>([&](auto S) {
      constexpr int s = S, nb = s >> 2, mb = s & 3;
      acc[mb][nb] = MFMA16(CAT(Bl[CUR][nb], Bh[CUR][nb]), Af[CUR][mb], acc[mb][nb]);
      if constexpr (s < 12) readAll(std::integral_constant<int, CUR ^ 1>{}, S, so1);
      if constexpr ((s & 3) == 1) issue(i + K2_NST, st, s >> 2);
      SCHED();
    });
    st = st1;
  };
  for (int i = 0; i < nt; i += 2) { body(I0{}, i); body(I1{}, i + 1); }
  WAIT_L(0); WAIT_V(0); BAR(); SCHED();
  STAMP_AT(3);
  if (role == 1) {
#pragma unroll
    for (int mb = 0; mb < 4; ++mb)
#pragma unroll
      for (int nb = 0; nb < 4; ++nb) acc[mb][nb] *= omg[mb];
  }
  const rsrc_t rp = mk_rsrc(A.ws + WS_PART);
  const unsigned poff = (unsigned)((tile * 8 + wid) * 8192 + lane * 16);
  u32x4 other[8];
  if (role == 0) {
#pragma unroll
    for (int n = 0; n < 8; ++n) other[n] = __builtin_amdgcn_raw_buffer_load_b128(rp, poff + n * 1024, 0, 16);
  }
  LAS char* ex = lds + (wid & 3) * 16384;
#pragma unroll
  for (int m2 = 0; m2 < 2; ++m2)
#pragma unroll
    for (int nb = 0; nb < 4; ++nb) {
      const f32x4 v = kh == 0 ? acc[2 + m2][nb] : acc[m2][nb];
      *(LAS f32x4*)(ex + kh * 8192 + (m2 * 4 + nb) * 1024 + lane * 16) = v;
    }
  WAIT_L(0); BAR(); SCHED();
  f32x4 fin[2][4];
#pragma unroll
  for (int m2 = 0; m2 < 2; ++m2)
#pragma unroll
    for (int nb = 0; nb < 4; ++nb) {
      const f32x4 o = *(LAS f32x4*)(ex + (kh ^ 1) * 8192 + (m2 * 4 + nb) * 1024 + lane * 16);
      fin[m2][nb] = (kh == 0 ? acc[m2][nb] : acc[2 + m2][nb]) + o;
    }
  STAMP_AT(4);
  if (role == 1) {
#pragma unroll
    for (int m2 = 0; m2 < 2; ++m2)
#pragma unroll
      for (int nb = 0; nb < 4; ++nb) __builtin_amdgcn_raw_buffer_store_b128(__builtin_bit_cast(u32x4, fin[m2][nb]), rp, poff + (m2 * 4 + nb) * 1024, 0, 16);
    WAIT_V(0);
    BAR();
  } else {
    const int chunk_g = (g4 & 1) * 2 + (g4 >> 1);
#pragma unroll
    for (int m2 = 0; m2 < 2; ++m2)
#pragma unroll
      for (int pp = 0; pp < 2; ++pp) {
        const f32x4 e = fin[m2][2 * pp] + __builtin_bit_cast(f32x4, other[m2 * 4 + 2 * pp]), o = fin[m2][2 * pp + 1] + __builtin_bit_cast(f32x4, other[m2 * 4 + 2 * pp + 1]);
        const unsigned e0 = cvt_pk(e[0], e[1]), e1 = cvt_pk(e[2], e[3]), o0 = cvt_pk(o[0], o[1]), o1 = cvt_pk(o[2], o[3]);
        const auto r0 = __builtin_amdgcn_permlane16_swap(e0, o0, false, false);
        const auto r1 = __builtin_amdgcn_permlane16_swap(e1, o1, false, false);
        const u32x4 w = {r0[0], r1[0], r0[1], r1[1]};
        *(u32x4*)(A.out + (size_t)(tm * 128 + 64 * wm + 32 * kh + 16 * m2 + l15) * 1024 + tn * 128 + 64 * wn + 32 * pp + chunk_g * 8) = w;
      }
  }
}

extern "C" void kernel_launch(void* const* d_in, const int* in_sizes, int n_in, void* d_out, int out_size, void* d_ws, size_t ws_size, hipStream_t stream) {
  K1Args a; a.x = (const u16*)d_in[0]; a.wqkv = (const u16*)d_in[1]; a.w1 = (const u16*)d_in[4]; a.b1 = (const u16*)d_in[5]; a.gw = (const u16*)d_in[7]; a.ws = (char*)d_ws; a.w2 = (const u16*)d_in[6]; a.wo = (const u16*)d_in[2];
  (void)hipFuncSetAttribute((const void*)k1_kernel, hipFuncAttributeMaxDynamicSharedMemorySize, K1_LDS);
  (void)hipFuncSetAttribute((const void*)k2_kernel, hipFuncAttributeMaxDynamicSharedMemorySize, K2_LDS);
  k1_kernel<<<256, 512, K1_LDS, stream>>>(a);
  K2Args c; c.w2 = (const u16*)d_in[6]; c.wo = (const u16*)d_in[2]; c.ws = (char*)d_ws; c.out = (u16*)d_out;
  k2_kernel<<<256, 576, K2_LDS, stream>>>(c);
}
```

```cpp
#include <hip/hip_runtime.h>
#include <stdint.h>
#include <utility>
#define ALT 3
#define LAS __attribute__((address_space(3)))
#define DI __device__ __forceinline__
typedef unsigned short u16;
typedef short bf16x8 __attribute__((ext_vector_type(8)));
typedef short s16x4 __attribute__((ext_vector_type(4)));
typedef float f32x4 __attribute__((ext_vector_type(4)));
typedef float f32x2 __attribute__((ext_vector_type(2)));
typedef unsigned u32x4 __attribute__((ext_vector_type(4)));
typedef unsigned u32x2 __attribute__((ext_vector_type(2)));
typedef __amdgpu_buffer_rsrc_t rsrc_t;

constexpr int XA_LD = 4608;
constexpr size_t WS_XA = 0;
constexpr size_t WS_PQ = WS_XA + (size_t)2048 * XA_LD * 2;
constexpr size_t WS_KV = WS_PQ + (size_t)2048 * 1024 * 2;
constexpr size_t WS_Z  = WS_KV + (size_t)8 * 32 * 1024 * 4;
constexpr size_t WS_G  = WS_Z + (size_t)8 * 32 * 32 * 4;
constexpr size_t WS_FL = WS_G + 2048 * 4;
constexpr int N_FLAG_WORDS = 64 * 137;
constexpr size_t WS_PART = WS_FL + 65536;

#define SCHED() __builtin_amdgcn_sched_barrier(0)
#define WAIT_V(n) asm volatile("s_waitcnt vmcnt(%0)" ::"n"(n) : "memory")
#define WAIT_L(n) asm volatile("s_waitcnt lgkmcnt(%0)" ::"n"(n) : "memory")
#define BAR() __builtin_amdgcn_s_barrier()

template <int OFF> DI void rd128(bf16x8& d, unsigned a) { asm volatile("ds_read_b128 %0, %1 offset:%2" : "=v"(d) : "v"(a), "i"(OFF) : "memory"); }
template <int OFF> DI void rdtr(s16x4& d, unsigned a) { asm volatile("ds_read_b64_tr_b16 %0, %1 offset:%2" : "=v"(d) : "v"(a), "i"(OFF) : "memory"); }
DI void gload1(float& d, const void* p) { asm volatile("global_load_dword %0, %1, off" : "=v"(d) : "v"(p) : "memory"); }
DI void gload2(u32x2& d, const void* p) { asm volatile("global_load_dwordx2 %0, %1, off" : "=v"(d) : "v"(p) : "memory"); }
template <class T> DI void pin(T& v) { asm volatile("" : "+v"(v)); }
template <class T> DI void keep(const T& v) { asm volatile("" :: "v"(v)); }
DI rsrc_t mk_rsrc(const void* p, unsigned n = 0x7fffffff) { return __builtin_amdgcn_make_buffer_rsrc((void*)p, 0, n, 0x00020000); }
DI void dma16(rsrc_t r, unsigned voff, unsigned soff, LAS char* l) { __builtin_amdgcn_raw_ptr_buffer_load_lds(r, (LAS void*)l, 16, voff, soff, 0, 0); }
DI unsigned cvt_pk(float lo, float hi) { unsigned r; asm("v_cvt_pk_bf16_f32 %0, %1, %2" : "=v"(r) : "v"(lo), "v"(hi)); return r; }
DI float bflo(unsigned w) { return __uint_as_float(w << 16); }
DI float bfhi(unsigned w) { return __uint_as_float(w & 0xffff0000u); }
constexpr float LOG2E = 1.44269504088896f;
DI float phi_f(float t) { const float e = __builtin_amdgcn_exp2f(__builtin_fminf(t * LOG2E, 0.f)); return e + __builtin_fmaxf(t + 1e-6f, 1e-6f); }
DI f32x2 mish2(f32x2 h) {
  const f32x2 t = h * LOG2E;
  f32x2 e; e.x = __builtin_amdgcn_exp2f(t.x); e.y = __builtin_amdgcn_exp2f(t.y);
  const f32x2 a = __builtin_elementwise_fma(e, (f32x2){0.5f, 0.5f}, (f32x2){1.f, 1.f});
  const f32x2 d = __builtin_elementwise_fma(e, a, (f32x2){1.f, 1.f});
  f32x2 r; r.x = __builtin_amdgcn_rcpf(d.x); r.y = __builtin_amdgcn_rcpf(d.y);
  return __builtin_elementwise_fma(-h, r, h);
}
DI f32x4 mish4(const f32x4& v) { const f32x2 a = mish2((f32x2){v[0], v[1]}), b = mish2((f32x2){v[2], v[3]}); return (f32x4){a.x, a.y, b.x, b.y}; }
DI f32x4 phi4(const f32x4& v) { return (f32x4){phi_f(v[0]), phi_f(v[1]), phi_f(v[2]), phi_f(v[3])}; }
#define MFMA16(a, b, c) __builtin_amdgcn_mfma_f32_16x16x32_bf16((a), (b), (c), 0, 0, 0)
#define CAT(L, H) __builtin_shufflevector((L), (H), 0, 1, 2, 3, 4, 5, 6, 7)
template <class F, int... I> DI void static_for_impl(F&& f, std::integer_sequence<int, I...>) { (f(std::integral_constant<int, I>{}), ...); }
template <int N, class F> DI void static_for(F&& f) { static_for_impl(f, std::make_integer_sequence<int, N>{}); }

constexpr int K1_XS = 32768, K1_WS = 26624, K1_WOFF = 3 * K1_XS, K1_DUMMY = K1_WOFF + 2 * K1_WS, K1_LDS = K1_DUMMY + 1024;

struct K1Args {
  const u16* x; const u16* wqkv; const u16* w1; const u16* b1; const u16* gw; char* ws; const u16* w2; const u16* wo;
};
DI void touch(const void* p, LAS char* dummy) { __builtin_amdgcn_global_load_lds((const unsigned*)p, (LAS unsigned*)dummy, 4, 0, 0); }

template <bool KV, int WN> constexpr int blk_panel(int b) { return b == 6 ? 3 : (KV ? (b >> 1) : (b < 4 ? WN : 2)); }
template <bool KV, int WN> constexpr int blk_cb(int b) { return b == 6 ? 0 : (KV ? 2 * WN + (b & 1) : (b < 4 ? b : 2 * WN + (b & 1))); }
template <bool KV, int WN> constexpr bool blk_std(int b) { return KV && b < 4; }
template <bool KV, int WN> constexpr bool blk_bias(int b) { return KV ? b >= 4 : (WN == 1 || b >= 4); }

template <bool KV, int WN> DI void k1_epilogue(const K1Args& A, LAS char* lds, f32x4 (&acc)[4][7], int panel, int hp, int wm);
template <bool KV, int WN>
DI void k1_body(const K1Args& A, LAS char* lds, int panel, int hp, int j32, int wm, int lane) {
  constexpr int NB = 7;
  const int wid = WN * 4 + wm;
#define STAMP_AT(i)
  const int G = hp >> 1, a = hp & 1;
  const int g4 = lane >> 4, l15 = lane & 15;
  const int c0 = (KV ? 1024 : 0) + 64 * hp;
  const int c1 = KV ? (2048 + 64 * hp) : (448 * G + 128 + 128 * a);
  const int c2 = KV ? (448 * G + 64 * a) : (448 * G + 128 + 128 * a + 64);
  const int c3 = 448 * G + 384 + (KV ? 0 : 32) + 16 * a;
  auto pcol = [&](int P) { return P == 0 ? c0 : P == 1 ? c1 : P == 2 ? c2 : c3; };
  f32x4 acc[4][NB];
  static_for<NB>([&](auto B) {
    constexpr int b = B;
    f32x4 init = {0.f, 0.f, 0.f, 0.f};
    if constexpr (blk_bias<KV, WN>(b)) {
      const u32x2 w = *(const u32x2*)((const char*)A.b1 + (pcol(blk_panel<KV, WN>(b)) + 16 * blk_cb<KV, WN>(b) + 4 * g4) * 2);
      init = (f32x4){bflo(w.x), bfhi(w.x), bflo(w.y), bfhi(w.y)};
    }
#pragma unroll
    for (int mb = 0; mb < 4; ++mb) acc[mb][b] = init;
  });
  const unsigned ldsb = (unsigned)(size_t)lds;
  unsigned xcur = ldsb + (64 * wm + l15) * 128 + ((g4 ^ ((lane >> 1) & 7)) * 16);
  const int q4 = l15 >> 2, p4 = lane & 3;
  const int sr = ((q4 >> 1) & 1) | ((g4 & 1) << 1);
  unsigned aw[4];
#pragma unroll
  for (int c = 0; c < 4; ++c) aw[c] = ldsb + K1_WOFF + (8 * g4 + q4) * 128 + ((c ^ sr) * 32) + p4 * 8;
  unsigned an[2];
#pragma unroll
  for (int t = 0; t < 2; ++t) an[t] = ldsb + K1_WOFF + 24576 + (8 * g4 + ((4 * t) ^ (4 * (g4 & 1))) + q4) * 32 + p4 * 8;

  bf16x8 Af[4]; s16x4 Bl[NB], Bh[NB];
  auto readA = [&](auto MB, unsigned addr) { rd128<MB * 2048>(Af[MB], addr); };
  auto readBh = [&](auto WP, auto KS, auto B, auto H) {
    constexpr int b = B, P = blk_panel<KV, WN>(b), c = blk_cb<KV, WN>(b), wo = WP * K1_WS;
    if constexpr (P < 3) { if constexpr (H == 0) rdtr<wo + P * 8192 + KS * 4096>(Bl[b], aw[c]); else rdtr<wo + P * 8192 + KS * 4096 + 512>(Bh[b], aw[c]); }
    else { if constexpr (H == 0) rdtr<wo + KS * 1024>(Bl[b], an[0]); else rdtr<wo + KS * 1024>(Bh[b], an[1]); }
  };
  auto mma = [&](auto B, auto MB) {
    constexpr int b = B, mb = MB;
    if constexpr (blk_std<KV, WN>(b)) acc[mb][b] = MFMA16(Af[mb], CAT(Bl[b], Bh[b]), acc[mb][b]);
    else acc[mb][b] = MFMA16(CAT(Bl[b], Bh[b]), Af[mb], acc[mb][b]);
  };
  using I0 = std::integral_constant<int, 0>; using I1 = std::integral_constant<int, 1>;
  using I2 = std::integral_constant<int, 2>; using I3 = std::integral_constant<int, 3>;
  using I5 = std::integral_constant<int, 5>; using I6 = std::integral_constant<int, 6>;
  auto h1_reads = [&](auto U, auto WP, auto KS, unsigned xa_) {
    constexpr int u = U;
    if constexpr (WN == 0) {
      if constexpr (u == 0) { readA(I0{}, xa_); readBh(WP, KS, I6{}, I0{}); }
      else if constexpr (u == 1) { readA(I1{}, xa_); readBh(WP, KS, I6{}, I1{}); }
      else readBh(WP, KS, std::integral_constant<int, ((u - 2) >> 1)>{}, std::integral_constant<int, (u & 1)>{});
    } else {
      if constexpr (u == 0) readA(I0{}, xa_);
      else if constexpr (u == 1) readA(I1{}, xa_);
      else if constexpr (u < 4) readBh(WP, KS, I6{}, std::integral_constant<int, (u & 1)>{});
      else readBh(WP, KS, std::integral_constant<int, ((u - 4) >> 1)>{}, std::integral_constant<int, (u & 1)>{});
    }
  };
  constexpr int L0 = WN == 0 ? 14 : 12, L1 = 26 - L0;
  auto step = [&](auto WP, auto KS, unsigned xcur, auto WPN, auto KSN, unsigned xnext, auto BARRIER) {
    static_for<L0>([&](auto S) {
      constexpr int s = S;
      if constexpr (s == 0 || s == 2 || s == 4) WAIT_L(8);
      if constexpr (s == 6) WAIT_L(6);
      if constexpr (s == 8) WAIT_L(4);
      if constexpr (s == 10) WAIT_L(2);
      if constexpr ((s & 1) == 0) SCHED();
      if constexpr (s < 12) mma(std::integral_constant<int, (s >> 1)>{}, std::integral_constant<int, (s & 1)>{}); else mma(I6{}, std::integral_constant<int, (s & 1)>{});
      if constexpr (s == 0) readBh(WP, KS, I5{}, I0{});
      if constexpr (s == 1) readBh(WP, KS, I5{}, I1{});
      if constexpr (s == 2) readA(I2{}, xcur);
      if constexpr (s == 3) readA(I3{}, xcur);
      SCHED();
    });
    WAIT_L(0);
    if constexpr (decltype(BARRIER)::value) {
      BAR();
      if constexpr (WN == 0) __builtin_amdgcn_s_setprio(2);
    }
    else { if constexpr (WN == 0) __builtin_amdgcn_s_setprio(0); }
    SCHED();
    static_for<L1>([&](auto U) {
      constexpr int u = U;
      if constexpr (WN == 0) mma(std::integral_constant<int, (u >> 1)>{}, std::integral_constant<int, 2 + (u & 1)>{});
      else { if constexpr (u < 2) mma(I6{}, std::integral_constant<int, 2 + u>{}); else mma(std::integral_constant<int, ((u - 2) >> 1)>{}, std::integral_constant<int, 2 + (u & 1)>{}); }
      h1_reads(U, WPN, KSN, xnext);
      SCHED();
    });
  };
  STAMP_AT(1);
  BAR(); SCHED();
  STAMP_AT(2);
  static_for<L1>([&](auto U) { h1_reads(U, I0{}, I0{}, xcur); });
  if constexpr (WN == 0) __builtin_amdgcn_s_setprio(2); else __builtin_amdgcn_s_setprio(1);
  int xs = 0;
  auto ktile = [&](auto WP, int t) {
    unsigned x1 = xcur ^ 64u, xn = xcur + (xs == 2 ? (unsigned)(-2 * K1_XS) : (unsigned)K1_XS);
    pin(x1); pin(xn);
    step(WP, I0{}, xcur, WP, I1{}, x1, std::false_type{});
    step(WP, I1{}, x1, std::integral_constant<int, WP ^ 1>{}, I0{}, xn, std::true_type{});
    xcur = xn; xs = xs == 2 ? 0 : xs + 1;
  };
#pragma unroll 1
  for (int t = 0; t < 16; t += 2) { ktile(I0{}, t); ktile(I1{}, t + 1); }
  SCHED(); WAIT_L(0); SCHED();
  __builtin_amdgcn_s_setprio(0);
  STAMP_AT(3);

  k1_epilogue<KV, WN>(A, lds, acc, panel, hp, wm);
}

template <bool KV, int WN>
DI void k1_epilogue(const K1Args& A, LAS char* lds, f32x4 (&acc)[4][7], int panel, int hp, int wm) {
  int lane = threadIdx.x & 63; asm volatile("" : "+v"(lane));
  const int g4 = lane >> 4, l15 = lane & 15;
  const int G = hp >> 1, a = hp & 1;
  const int c0 = (KV ? 1024 : 0) + 64 * hp;
  const int c1 = KV ? (2048 + 64 * hp) : (448 * G + 128 + 128 * a);
  const int c2 = KV ? (448 * G + 64 * a) : (448 * G + 128 + 128 * a + 64);
  const int c3 = 448 * G + 384 + (KV ? 0 : 32) + 16 * a;
  using I0 = std::integral_constant<int, 0>; using I1 = std::integral_constant<int, 1>;
  u16* xa = (u16*)(A.ws + WS_XA);
  const size_t rowbase = (size_t)panel * 256 + 64 * wm;
  LAS char* stg = lds + (WN * 4 + wm) * 8192;
  const int chunk_g = (g4 & 1) * 2 + (g4 >> 1);
  auto store_line = [&](auto B0, auto ACT, u16* base, size_t ld, int col0) {
    static_for<4>([&](auto MB) {
      constexpr int mb = MB;
      static_for<2>([&](auto PP) {
        constexpr int p = PP;
        f32x4 e = acc[mb][B0 + 2 * p], o = acc[mb][B0 + 2 * p + 1];
        if constexpr (ACT == 0) { e = phi4(e); o = phi4(o); } else { e = mish4(e); o = mish4(o); }
        const unsigned e0 = cvt_pk(e[0], e[1]), e1 = cvt_pk(e[2], e[3]), o0 = cvt_pk(o[0], o[1]), o1 = cvt_pk(o[2], o[3]);
        const auto r0 = __builtin_amdgcn_permlane16_swap(e0, o0, false, false);
        const auto r1 = __builtin_amdgcn_permlane16_swap(e1, o1, false, false);
        const u32x4 w = {r0[0], r1[0], r0[1], r1[1]};
        *(LAS u32x4*)(stg + (16 * mb + l15) * 128 + (((4 * p + chunk_g) ^ (l15 & 7)) * 16)) = w;
      });
    });
#pragma unroll
    for (int i = 0; i < 8; ++i) {
      const u32x4 w = *(LAS u32x4*)(stg + i * 1024 + lane * 16);
      const int r = 8 * i + (lane >> 3), ch = (lane & 7) ^ (r & 7);
      *(u32x4*)(base + (rowbase + r) * ld + col0 + ch * 8) = w;
    }
  };
  auto store_half = [&](int col0) {
#pragma unroll
    for (int mb = 0; mb < 4; ++mb) {
      const f32x4 e = mish4(acc[mb][4]), o = mish4(acc[mb][5]);
      const unsigned e0 = cvt_pk(e[0], e[1]), e1 = cvt_pk(e[2], e[3]), o0 = cvt_pk(o[0], o[1]), o1 = cvt_pk(o[2], o[3]);
      const auto r0 = __builtin_amdgcn_permlane16_swap(e0, o0, false, false);
      const auto r1 = __builtin_amdgcn_permlane16_swap(e1, o1, false, false);
      const u32x4 w = {r0[0], r1[0], r0[1], r1[1]};
      *(u32x4*)(xa + (rowbase + 16 * mb + l15) * XA_LD + col0 + chunk_g * 8) = w;
    }
  };
  auto store_narrow = [&]() {
#pragma unroll
    for (int m2 = 0; m2 < 2; ++m2) {
      const int mb = 2 * WN + m2;
      const f32x4 v = mish4(acc[2 * WN + m2][6]);
      const u32x2 w = {cvt_pk(v[0], v[1]), cvt_pk(v[2], v[3])};
      *(u32x2*)(xa + (rowbase + 16 * mb + l15) * XA_LD + c3 + 4 * g4) = w;
    }
  };
  store_half(c2 + 32 * WN);
  store_narrow();
  if constexpr (!KV) {
    if constexpr (WN == 0) store_line(I0{}, I0{}, (u16*)(A.ws + WS_PQ), 1024, c0);
    else store_line(I0{}, I1{}, xa, XA_LD, c1);
  } else {
    f32x4 kvD[4]; f32x4 zD[2];
#pragma unroll
    for (int i = 0; i < 4; ++i) kvD[i] = (f32x4){0.f, 0.f, 0.f, 0.f};
    zD[0] = zD[1] = (f32x4){0.f, 0.f, 0.f, 0.f};
    const bf16x8 ones = {0x3f80, 0x3f80, 0x3f80, 0x3f80, 0x3f80, 0x3f80, 0x3f80, 0x3f80};
#pragma unroll
    for (int mp = 0; mp < 2; ++mp) {
      bf16x8 ka[2], vb[2];
#pragma unroll
      for (int b = 0; b < 2; ++b) {
        const f32x4 k0 = phi4(acc[2 * mp][b]), k1 = phi4(acc[2 * mp + 1][b]);
        const u32x4 w = {cvt_pk(k0[0], k0[1]), cvt_pk(k0[2], k0[3]), cvt_pk(k1[0], k1[1]), cvt_pk(k1[2], k1[3])};
        ka[b] = __builtin_bit_cast(bf16x8, w);
        const f32x4 v0 = acc[2 * mp][2 + b], v1 = acc[2 * mp + 1][2 + b];
        const u32x4 u = {cvt_pk(v0[0], v0[1]), cvt_pk(v0[2], v0[3]), cvt_pk(v1[0], v1[1]), cvt_pk(v1[2], v1[3])};
        vb[b] = __builtin_bit_cast(bf16x8, u);
      }
#pragma unroll
      for (int db = 0; db < 2; ++db) {
#pragma unroll
        for (int eb = 0; eb < 2; ++eb) kvD[db * 2 + eb] = MFMA16(ka[db], vb[eb], kvD[db * 2 + eb]);
        zD[db] = MFMA16(ka[db], ones, zD[db]);
      }
    }
    LAS char* sc = lds + 2 * K1_XS;
    LAS char* sz = lds + K1_WOFF;
#pragma unroll
    for (int i = 0; i < 4; ++i) *(LAS f32x4*)(sc + ((WN * 4 + wm) * 4 + i) * 1024 + lane * 16) = kvD[i];
    if (l15 == 0) {
#pragma unroll
      for (int db = 0; db < 2; ++db) *(LAS f32x4*)(sz + ((WN * 4 + wm) * 2 + db) * 64 + g4 * 16) = zD[db];
    }
    WAIT_L(0); BAR(); SCHED();
    const int blk = wm;
    f32x4 s = {0.f, 0.f, 0.f, 0.f};
#pragma unroll
    for (int m = 0; m < 4; ++m) s += *(LAS f32x4*)(sc + ((WN * 4 + m) * 4 + blk) * 1024 + lane * 16);
    float* kvp = (float*)(A.ws + WS_KV) + ((size_t)panel * 32 + 2 * hp + WN) * 1024;
    *(f32x4*)(kvp + (16 * (blk & 1) + l15) * 32 + 16 * (blk >> 1) + 4 * g4) = s;
    if (wm == 0 && lane < 8) {
      const int db = lane >> 2, gq = lane & 3;
      f32x4 zs = {0.f, 0.f, 0.f, 0.f};
#pragma unroll
      for (int m = 0; m < 4; ++m) zs += *(LAS f32x4*)(sz + ((WN * 4 + m) * 2 + db) * 64 + gq * 16);
      *(f32x4*)((float*)(A.ws + WS_Z) + ((size_t)panel * 32 + 2 * hp + WN) * 32 + 16 * db + 4 * gq) = zs;
    }
  }
}

DI void k1_loader(const K1Args& A, LAS char* lds, bool kv, int panel, int hp, int j32, bool isx, int lane) {
  const int G = hp >> 1, a = hp & 1;
  const int c0 = (kv ? 1024 : 0) + 64 * hp;
  const int c1 = kv ? (2048 + 64 * hp) : (448 * G + 128 + 128 * a);
  const int c2 = kv ? (448 * G + 64 * a) : (448 * G + 128 + 128 * a + 64);
  const int c3 = 448 * G + 384 + (kv ? 0 : 32) + 16 * a;
  unsigned vA[4], vB[4], vN[2];
#pragma unroll
  for (int k = 0; k < 4; ++k) {
    const int par = k & 1;
    const unsigned chx = (unsigned)(((lane & 7) ^ ((4 * par + (lane >> 4)) & 7)) * 16), chw = (unsigned)(((lane & 7) ^ ((((lane >> 4) & 1) | (par << 1)) << 1)) * 16);
    vA[k] = (isx ? (unsigned)((8 * k + (lane >> 3)) * 2048) + chx : (unsigned)((8 * k + (lane >> 3)) * 6144) + chw) + 3072 - k * 1024;
    vB[k] = (unsigned)((8 * k + (lane >> 3)) * 7168) + chw + 3072 - k * 1024;
  }
#pragma unroll
  for (int n = 0; n < 2; ++n) { const int rho = 32 * n + (lane >> 1); vN[n] = (unsigned)((rho ^ (((rho >> 3) & 1) << 2)) * 7168 + (lane & 1) * 16) + 3072 - n * 1024; }
  const rsrc_t rx = mk_rsrc((const char*)A.x - 3072), rq = mk_rsrc((const char*)A.wqkv - 3072), r1 = mk_rsrc((const char*)A.w1 - 3072);
  const unsigned xrow0 = (unsigned)panel * 256 * 2048;
  auto issueX = [&](int t, int xs) {
#pragma unroll
    for (int g = 0; g < 8; ++g)
      static_for<4>([&](auto K) { __builtin_amdgcn_raw_ptr_buffer_load_lds(rx, (LAS void*)(lds + xs * K1_XS + g * 4096), 16, vA[K], xrow0 + t * 128 + g * 65536, K * 1024, 0); });
  };
  auto issueW = [&](int t) {
    LAS char* dst = lds + K1_WOFF + (t & 1) * K1_WS;
#pragma unroll
    for (int g = 0; g < 2; ++g)
      static_for<4>([&](auto K) { __builtin_amdgcn_raw_ptr_buffer_load_lds(rq, (LAS void*)(dst + g * 4096), 16, vA[K], (t * 64 + g * 32) * 6144 + c0 * 2, K * 1024, 0); });
    if (kv) {
#pragma unroll
      for (int g = 0; g < 2; ++g)
        static_for<4>([&](auto K) { __builtin_amdgcn_raw_ptr_buffer_load_lds(rq, (LAS void*)(dst + 8192 + g * 4096), 16, vA[K], (t * 64 + g * 32) * 6144 + c1 * 2, K * 1024, 0); });
    } else {
#pragma unroll
      for (int g = 0; g < 2; ++g)
        static_for<4>([&](auto K) { __builtin_amdgcn_raw_ptr_buffer_load_lds(r1, (LAS void*)(dst + 8192 + g * 4096), 16, vB[K], (t * 64 + g * 32) * 7168 + c1 * 2, K * 1024, 0); });
    }
#pragma unroll
    for (int g = 0; g < 2; ++g)
      static_for<4>([&](auto K) { __builtin_amdgcn_raw_ptr_buffer_load_lds(r1, (LAS void*)(dst + 16384 + g * 4096), 16, vB[K], (t * 64 + g * 32) * 7168 + c2 * 2, K * 1024, 0); });
    static_for<2>([&](auto N) { __builtin_amdgcn_raw_ptr_buffer_load_lds(r1, (LAS void*)(dst + 24576), 16, vN[N], t * 64 * 7168 + c3 * 2, N * 1024, 0); });
  };
  unsigned tdummy = 0;
  auto touch3 = [&]() {
    const int w = blockIdx.x * 2 + (isx ? 0 : 1);
#pragma unroll
    for (int n = 0; n < 3; ++n) {
      const int k = n * 64 + lane, li = w * 144 + (k < 144 ? k : 143);
      const char* p = li < 57344 ? (const char*)A.w2 + (size_t)li * 128 : (const char*)A.wo + (size_t)(li - 57344) * 128;
      asm volatile("global_load_dword %0, %1, off" : "+v"(tdummy) : "v"(p) : "memory");
    }
  };
  if (isx) {
    issueX(0, 0); issueX(1, 1);
#pragma unroll
    for (int n = 0; n < 4; ++n)
      asm volatile("global_load_dword %0, %1, off" : "+v"(tdummy) : "v"((const char*)A.x + (size_t)(panel * 256 + 16 * (j32 & 15) + 4 * n + (lane >> 4)) * 2048 + (lane & 15) * 128) : "memory");
    WAIT_V(36); BAR();
    issueX(2, 2);
    int xs = 0;
#pragma unroll 1
    for (int t = 0; t < 13; ++t) {
      WAIT_V(32); BAR(); issueX(t + 3, xs); xs = xs == 2 ? 0 : xs + 1;
      if (t < 7) {
        const int li = blockIdx.x * 52 + (lane < 52 ? lane : 51);
        int row = 64 * (2 * t + 3) + li / 104; row = row < 1024 ? row : 1023;
        const int c = li % 104;
        const char* p = c < 48 ? (const char*)A.wqkv + (size_t)row * 6144 + c * 128 : (const char*)A.w1 + (size_t)row * 7168 + (c - 48) * 128;
        asm volatile("global_load_dword %0, %1, off" : "+v"(tdummy) : "v"(p) : "memory");
      }
    }
    touch3();
    WAIT_V(35); BAR();
    WAIT_V(3); BAR();
    BAR();
  } else {
    issueW(0); issueW(1);
    WAIT_V(26); BAR();
#pragma unroll 1
    for (int t = 0; t < 14; ++t) { WAIT_V(0); BAR(); issueW(t + 2); }
    touch3();
    WAIT_V(3); BAR();
    BAR();
  }
#pragma unroll 1
  for (int r = 0; r < 4; ++r) {
    const int row = panel * 256 + j32 * 8 + (isx ? 0 : 4) + r;
    const u32x4* xr = (const u32x4*)((const char*)A.x + (size_t)row * 2048 + lane * 32);
    const u32x4* gr = (const u32x4*)((const char*)A.gw + lane * 32);
    const u32x4 gx0 = xr[0], gx1 = xr[1], gg0 = gr[0], gg1 = gr[1];
    float s = 0.f;
#pragma unroll
    for (int i = 0; i < 4; ++i) {
      s = __builtin_fmaf(bflo(gx0[i]), bflo(gg0[i]), s); s = __builtin_fmaf(bfhi(gx0[i]), bfhi(gg0[i]), s);
      s = __builtin_fmaf(bflo(gx1[i]), bflo(gg1[i]), s); s = __builtin_fmaf(bfhi(gx1[i]), bfhi(gg1[i]), s);
    }
#pragma unroll
    for (int o = 32; o >= 1; o >>= 1) s += __shfl_xor(s, o);
    const float gval = __builtin_amdgcn_rcpf(1.f + __builtin_amdgcn_exp2f(-LOG2E * s));
    if (lane == 0) ((float*)(A.ws + WS_G))[row] = gval;
  }
  if (kv) BAR();
  WAIT_V(0); keep(tdummy);
}

__global__ __launch_bounds__(640, 3) void k1_kernel(K1Args A) {
  extern __shared__ __attribute__((aligned(16))) char smem[];
  LAS char* lds = (LAS char*)smem;
  const int b = blockIdx.x, xcd = b & 7, idx = b >> 3, rq = xcd >> 1, ch = xcd & 1;
  const int panel = 2 * rq + (idx >> 4), c16 = idx & 15, hp = 8 * ch + (c16 >> 1), j32 = ch * 16 + c16;
  const int wid = __builtin_amdgcn_readfirstlane(threadIdx.x >> 6), lane = threadIdx.x & 63;
  if (b == 0 && threadIdx.x < 137) ((unsigned*)(A.ws + WS_FL))[threadIdx.x * 64] = 0u;
  if (wid >= 8) { k1_loader(A, lds, (c16 & 1) != 0, panel, hp, j32, wid == 8, lane); return; }
  if (c16 & 1) {
    if (wid < 4) k1_body<true, 0>(A, lds, panel, hp, j32, wid, lane); else k1_body<true, 1>(A, lds, panel, hp, j32, wid - 4, lane);
  } else {
    if (wid < 4) k1_body<false, 0>(A, lds, panel, hp, j32, wid, lane); else k1_body<false, 1>(A, lds, panel, hp, j32, wid - 4, lane);
  }
}

#define K2_NA 26
constexpr int K2_ST = 32768, K2_SCR = 4 * K2_ST, K2_LDS = 4 * K2_ST + 4096;
struct K2Args { const u16* w2; const u16* wo; char* ws; u16* out; };
typedef __attribute__((address_space(1))) unsigned gu32;
#define RLX_AGENT __ATOMIC_RELAXED, __HIP_MEMORY_SCOPE_AGENT
DI void dma16_sc1(rsrc_t r, unsigned voff, unsigned soff, LAS char* l) { __builtin_amdgcn_raw_ptr_buffer_load_lds(r, (LAS void*)l, 16, voff, soff, 0, 16); }
DI void poll_issue(unsigned& d, const gu32* p) { asm volatile("global_load_dword %0, %1, off sc1" : "=v"(d) : "v"(p) : "memory"); }
DI bool wait_ge(gu32* word, unsigned want, gu32* tmo) {
  for (unsigned spins = 0;; ++spins) {
    if ((unsigned)__builtin_amdgcn_readfirstlane(__hip_atomic_load(word, RLX_AGENT)) >= want) return true;
    if (spins > (1u << 20)) { __hip_atomic_store(tmo, 1u, RLX_AGENT); return false; }
    __builtin_amdgcn_s_sleep(2);
  }
}

__global__ __launch_bounds__(640, 3) void k2_kernel(K2Args A) {
  extern __shared__ __attribute__((aligned(16))) char smem[];
  LAS char* lds = (LAS char*)smem;
  const int b = blockIdx.x, xcd = b & 7, idx = b >> 3, rq = xcd >> 1, role = xcd & 1;
  const int tm = 4 * rq + (idx >> 3), tn = idx & 7;
  const int wid = __builtin_amdgcn_readfirstlane(threadIdx.x >> 6), lane = threadIdx.x & 63;
  gu32* flags = (gu32*)(A.ws + WS_FL);
  const int tile = tm * 8 + tn;
  const int pp_ = 2 * rq + (idx >> 4), hh = 16 * role + (idx & 15);
  const int nt = role == 0 ? K2_NA + 16 : 56 - K2_NA;
  if (wid >= 8) {
    const bool isx = wid == 8;
    unsigned voff[4];
#pragma unroll
    for (int k = 0; k < 4; ++k) {
      const int par = k & 1;
      const int swx = (4 * par + (lane >> 4)) & 7, sww = (((lane >> 4) & 1) | (par << 1)) << 1;
      voff[k] = (isx ? (unsigned)((8 * k + (lane >> 3)) * (XA_LD * 2) + (((lane & 7) ^ swx) * 16)) : (unsigned)((8 * k + (lane >> 3)) * 2048 + (((lane & 7) ^ sww) * 16))) + 3072 - k * 1024;
    }
    const unsigned gstep = isx ? 32 * XA_LD * 2 : 32 * 2048;
    auto issue = [&](rsrc_t r, unsigned so, int stage) {
      LAS char* st = lds + stage * K2_ST + (isx ? 0 : 16384);
#pragma unroll
      for (int g = 0; g < 4; ++g) {
        const unsigned sg = isx ? so + g * gstep : so + (g & 1) * gstep + (g >> 1) * 128;
        static_for<4>([&](auto K) { __builtin_amdgcn_raw_ptr_buffer_load_lds(r, (LAS void*)(st + g * 4096), 16, voff[K], sg, K * 1024, 16); });
      }
    };
    const rsrc_t r0 = mk_rsrc(isx ? (const char*)(A.ws + WS_XA) - 3072 : (const char*)A.w2 - 3072);
    const rsrc_t r1 = mk_rsrc(isx ? (const char*)(A.ws + WS_XA) - 3072 : (const char*)A.wo - 3072);
    const unsigned tstep = isx ? 128u : 64u * 2048u;
    const unsigned sbase = isx ? (unsigned)tm * 128 * (XA_LD * 2) : (unsigned)tn * 256;
    const unsigned s0 = sbase + (role == 0 ? 0 : K2_NA) * tstep;
    const unsigned s1 = isx ? sbase + 56 * tstep : sbase;
    const rsrc_t rp = mk_rsrc(A.ws + WS_PART);
    auto issue_partial = [&]() {
#pragma unroll
      for (int half = 0; half < 2; ++half)
#pragma unroll
        for (int g = 0; g < 4; ++g)
          static_for<4>([&](auto K) { __builtin_amdgcn_raw_ptr_buffer_load_lds(rp, (LAS void*)(lds + ((nt + half) & 3) * K2_ST + (isx ? 0 : 16384) + g * 4096), 16, lane * 16,
                                                                                (unsigned)tile * 65536 + half * 32768 + (isx ? 0 : 16384) + g * 4096, K * 1024, 16); });
    };
    issue(r0, s0, 0); issue(r0, s0 + tstep, 1); issue(r0, s0 + 2 * tstep, 2);
    WAIT_V(32); BAR();
    issue(r0, s0 + 3 * tstep, 3);
    auto run = [&](int i0, int i1, rsrc_t r, unsigned so) {
#pragma unroll 1
      for (int i = i0; i < i1; ++i, so += tstep) { WAIT_V(32); BAR(); issue(r, so, i & 3); }
    };
    const int nA = role == 0 ? K2_NA : nt;
    unsigned pv = 0;
    run(0, 5, r0, s0 + 4 * tstep);
    if (isx && lane == 0) __hip_atomic_fetch_add(flags + 64 * pp_, 1u, RLX_AGENT);
    if (role == 0) {
      run(5, K2_NA - 8, r0, s0 + 9 * tstep);
      if (isx) poll_issue(pv, flags + 64 * (tm >> 1));
      run(K2_NA - 8, K2_NA - 4, r0, s0 + (K2_NA - 4) * tstep);
      if (isx) {
        WAIT_V(32); pin(pv);
        if ((unsigned)__builtin_amdgcn_readfirstlane(pv) < 32u) wait_ge(flags + 64 * (tm >> 1), 32u, flags + 64 * 136);
      }
      run(K2_NA - 4, nt - 4, r1, s1);
    } else run(5, nt - 4, r0, s0 + 9 * tstep);
    poll_issue(pv, flags + 64 * (8 + tile));
    WAIT_V(33); BAR();
    WAIT_V(17); BAR();
    WAIT_V(0);
    bool early = false;
    if (role == 0) { pin(pv); early = (unsigned)__builtin_amdgcn_readfirstlane(pv) != 0u; }
    BAR();
    if (early) issue_partial();
    BAR();
    (void)nA;
    if (role == 0) {
      if (!early) wait_ge(flags + 64 * (8 + tile), 1u, flags + 64 * 136);
      BAR();
      if (!early) issue_partial();
      WAIT_V(0);
      BAR();
    } else {
      BAR();
      if (isx && lane == 0) __hip_atomic_store(flags + 64 * (8 + tile), 1u, RLX_AGENT);
    }
    return;
  }
  const int wm = wid >> 2, wn = wid & 3;
  const int g4 = lane >> 4, l15 = lane & 15;
  const size_t prow = (size_t)pp_ * 256 + 32 * wid;
  f32x2 part[8]; bf16x8 pf[2]; float gv[2], gq[4]; f32x4 zq;
  {
    const float* kvp = (const float*)(A.ws + WS_KV) + (size_t)hh * 1024 + (wid * 64 + lane) * 2;
#pragma unroll
    for (int q = 0; q < 8; ++q) part[q] = *(const f32x2*)(kvp + (size_t)q * 32 * 1024);
    zq = *(const f32x4*)((const float*)(A.ws + WS_Z) + (size_t)(lane >> 3) * 1024 + hh * 32 + (lane & 7) * 4);
#pragma unroll
    for (int mb = 0; mb < 2; ++mb) {
      pf[mb] = *(const bf16x8*)((const u16*)(A.ws + WS_PQ) + (prow + 16 * mb + l15) * 1024 + 32 * hh + 8 * g4);
      gv[mb] = ((const float*)(A.ws + WS_G))[prow + 16 * mb + l15];
    }
#pragma unroll
    for (int mb = 0; mb < 4; ++mb) gq[mb] = ((const float*)(A.ws + WS_G))[tm * 128 + 64 * wm + 16 * mb + l15];
  }
  const unsigned ldsb = (unsigned)(size_t)lds;
  unsigned ax[2];
#pragma unroll
  for (int ks = 0; ks < 2; ++ks) ax[ks] = ldsb + (64 * wm + l15) * 128 + (((4 * ks + g4) ^ ((lane >> 1) & 7)) * 16);
  const int q4 = l15 >> 2, p4 = lane & 3;
  const int sr = ((q4 >> 1) & 1) | ((g4 & 1) << 1);
  unsigned aw[2];
#pragma unroll
  for (int nb = 0; nb < 2; ++nb) aw[nb] = ldsb + 16384 + (wn >> 1) * 8192 + (8 * g4 + q4) * 128 + (((2 * (wn & 1) + nb) ^ sr) * 32) + p4 * 8;
  f32x4 acc[4][2];
#pragma unroll
  for (int i = 0; i < 4; ++i) acc[i][0] = acc[i][1] = (f32x4){0.f, 0.f, 0.f, 0.f};
  bf16x8 Af[2][2][4]; s16x4 Bl[2][2][2], Bh[2][2][2];
  auto rd = [&](auto BUF, auto I, const unsigned (&pa)[2], const unsigned (&pw)[2]) {
    constexpr int i = I, ks = i >> 3, r = i & 7;
    if constexpr (r < 2) { if constexpr (r == 0) rdtr<ks * 4096>(Bl[BUF][ks][0], pw[0]); else rdtr<ks * 4096 + 512>(Bh[BUF][ks][0], pw[0]); }
    else if constexpr (r < 6) rd128<(r - 2) * 2048>(Af[BUF][ks][r - 2], pa[ks]);
    else { if constexpr (r == 6) rdtr<ks * 4096>(Bl[BUF][ks][1], pw[1]); else rdtr<ks * 4096 + 512>(Bh[BUF][ks][1], pw[1]); }
  };
  using I0 = std::integral_constant<int, 0>; using I1 = std::integral_constant<int, 1>;
  STAMP_AT(1);
  BAR(); SCHED();
  STAMP_AT(2);
  LAS char* kvs = lds + K2_SCR;
  {
    const f32x2 s2 = ((part[0] + part[1]) + (part[2] + part[3])) + ((part[4] + part[5]) + (part[6] + part[7]));
    *(LAS unsigned*)(kvs + (wid * 64 + lane) * 4) = cvt_pk(s2.x, s2.y);
    if (wid == 0) {
      f32x4 z = zq;
#pragma unroll
      for (int o = 8; o <= 32; o <<= 1) { z[0] += __shfl_xor(z[0], o); z[1] += __shfl_xor(z[1], o); z[2] += __shfl_xor(z[2], o); z[3] += __shfl_xor(z[3], o); }
      if (lane < 8) *(LAS u32x2*)(kvs + 2048 + lane * 8) = (u32x2){cvt_pk(z[0], z[1]), cvt_pk(z[2], z[3])};
    }
    SCHED();
  }
  static_for<16>([&](auto I) { rd(I0{}, I, ax, aw); });
  auto body = [&](auto CUR, int i) {
    constexpr int cur = CUR;
    const unsigned so1 = ((i + 1) & 3) * K2_ST;
    unsigned pa[2] = {ax[0] + so1, ax[1] + so1}, pw[2] = {aw[0] + so1, aw[1] + so1};
    pin(pa[0]); pin(pa[1]); pin(pw[0]); pin(pw[1]);
    WAIT_L(0); BAR(); SCHED();
    static_for<16>([&](auto S) {
      constexpr int s = S, ks = s >> 3, nb = (s >> 2) & 1, mb = s & 3;
      acc[mb][nb] = MFMA16(CAT(Bl[cur][ks][nb], Bh[cur][ks][nb]), Af[cur][ks][mb], acc[mb][nb]);
      if constexpr (s < 8) { rd(std::integral_constant<int, cur ^ 1>{}, S, pa, pw); }
      else if constexpr (s < 12) { rd(std::integral_constant<int, cur ^ 1>{}, std::integral_constant<int, 2 * s - 8>{}, pa, pw); rd(std::integral_constant<int, cur ^ 1>{}, std::integral_constant<int, 2 * s - 7>{}, pa, pw); }
      SCHED();
    });
  };
  body(I0{}, 0);
  {
    bf16x8 ka[2], za;
    const unsigned kb = (unsigned)(size_t)kvs;
    rd128<0>(ka[0], kb + l15 * 64 + g4 * 16); rd128<1024>(ka[1], kb + l15 * 64 + g4 * 16); rd128<2048>(za, kb + g4 * 16);
    WAIT_L(0); SCHED();
    const rsrc_t ro = mk_rsrc(A.ws + WS_XA);
    const int chunk_g = (g4 & 1) * 2 + (g4 >> 1);
#pragma unroll
    for (int mb = 0; mb < 2; ++mb) {
      const f32x4 zero = {0.f, 0.f, 0.f, 0.f};
      const f32x4 n0 = MFMA16(ka[0], pf[mb], zero), n1 = MFMA16(ka[1], pf[mb], zero), dn = MFMA16(za, pf[mb], zero);
      const float sc = gv[mb] * __builtin_amdgcn_rcpf(dn[0] + 1e-6f);
      const f32x4 e = n0 * sc, o = n1 * sc;
      const unsigned e0 = cvt_pk(e[0], e[1]), e1 = cvt_pk(e[2], e[3]), o0 = cvt_pk(o[0], o[1]), o1 = cvt_pk(o[2], o[3]);
      const auto r0 = __builtin_amdgcn_permlane16_swap(e0, o0, false, false);
      const auto r1 = __builtin_amdgcn_permlane16_swap(e1, o1, false, false);
      const u32x4 w = {r0[0], r1[0], r0[1], r1[1]};
      const unsigned off = (unsigned)(((prow + 16 * mb + l15) * XA_LD + 3584 + 32 * hh + chunk_g * 8) * 2);
      __builtin_amdgcn_raw_buffer_store_b128(w, ro, off, 0, 16);
    }
    SCHED();
  }
  body(I1{}, 1);
  body(I0{}, 2);
  float omg[4];
#pragma unroll
  for (int mb = 0; mb < 4; ++mb) omg[mb] = 1.f - gq[mb];
  body(I1{}, 3);
  WAIT_V(0);
  WAIT_L(0); SCHED();
  const int n1 = role == 0 ? K2_NA : nt;
  for (int i = 4; i < n1; i += 2) { body(I0{}, i); body(I1{}, i + 1); }
  SCHED(); WAIT_L(0); SCHED();
#pragma unroll
  for (int mb = 0; mb < 4; ++mb) { acc[mb][0] *= omg[mb]; acc[mb][1] *= omg[mb]; }
  for (int i = n1; i < nt; i += 2) { body(I0{}, i); body(I1{}, i + 1); }
  SCHED(); WAIT_L(0); SCHED();
  STAMP_AT(3);
  if (role == 1) {
    const rsrc_t rp = mk_rsrc(A.ws + WS_PART);
    const unsigned poff = (unsigned)((tile * 8 + wid) * 8192 + lane * 16);
#pragma unroll
    for (int mb = 0; mb < 4; ++mb)
#pragma unroll
      for (int nb = 0; nb < 2; ++nb) __builtin_amdgcn_raw_buffer_store_b128(__builtin_bit_cast(u32x4, acc[mb][nb]), rp, poff + (mb * 2 + nb) * 1024, 0, 16);
    STAMP_AT(4);
    WAIT_V(0);
    BAR();
  } else {
    BAR();
    BAR();
    STAMP_AT(4);
    const int chunk_g = (g4 & 1) * 2 + (g4 >> 1);
    LAS char* pl = lds + ((nt + (wid >> 2)) & 3) * K2_ST + (wid & 3) * 8192 + lane * 16;
#pragma unroll
    for (int mb = 0; mb < 4; ++mb) {
      const f32x4 e = acc[mb][0] + *(LAS f32x4*)(pl + (mb * 2) * 1024), o = acc[mb][1] + *(LAS f32x4*)(pl + (mb * 2 + 1) * 1024);
      const unsigned e0 = cvt_pk(e[0], e[1]), e1 = cvt_pk(e[2], e[3]), o0 = cvt_pk(o[0], o[1]), o1 = cvt_pk(o[2], o[3]);
      const auto r0 = __builtin_amdgcn_permlane16_swap(e0, o0, false, false);
      const auto r1 = __builtin_amdgcn_permlane16_swap(e1, o1, false, false);
      const u32x4 w = {r0[0], r1[0], r0[1], r1[1]};
      *(u32x4*)(A.out + (size_t)(tm * 128 + 64 * wm + 16 * mb + l15) * 1024 + tn * 128 + 32 * wn + chunk_g * 8) = w;
    }
  }
}

extern "C" void kernel_launch(void* const* d_in, const int* in_sizes, int n_in, void* d_out, int out_size, void* d_ws, size_t ws_size, hipStream_t stream) {
  K1Args a; a.x = (const u16*)d_in[0]; a.wqkv = (const u16*)d_in[1]; a.w1 = (const u16*)d_in[4]; a.b1 = (const u16*)d_in[5]; a.gw = (const u16*)d_in[7]; a.ws = (char*)d_ws; a.w2 = (const u16*)d_in[6]; a.wo = (const u16*)d_in[2];
  (void)hipFuncSetAttribute((const void*)k1_kernel, hipFuncAttributeMaxDynamicSharedMemorySize, K1_LDS);
  (void)hipFuncSetAttribute((const void*)k2_kernel, hipFuncAttributeMaxDynamicSharedMemorySize, K2_LDS);
  k1_kernel<<<256, 640, K1_LDS, stream>>>(a);
  K2Args c; c.w2 = (const u16*)d_in[6]; c.wo = (const u16*)d_in[2]; c.ws = (char*)d_ws; c.out = (u16*)d_out;
  k2_kernel<<<256, 640, K2_LDS, stream>>>(c);
}
```

```cpp
#include <hip/hip_runtime.h>
#include <stdint.h>
#include <utility>
#define ALT 3
#define K1_AUX 16
#define LAS __attribute__((address_space(3)))
#define DI __device__ __forceinline__
typedef unsigned short u16;
typedef short bf16x8 __attribute__((ext_vector_type(8)));
typedef short s16x4 __attribute__((ext_vector_type(4)));
typedef float f32x4 __attribute__((ext_vector_type(4)));
typedef float f32x2 __attribute__((ext_vector_type(2)));
typedef unsigned u32x4 __attribute__((ext_vector_type(4)));
typedef unsigned u32x2 __attribute__((ext_vector_type(2)));
typedef __amdgpu_buffer_rsrc_t rsrc_t;

constexpr int XA_LD = 4608;
constexpr size_t WS_XA = 0;
constexpr size_t WS_PQ = WS_XA + (size_t)2048 * XA_LD * 2;
constexpr size_t WS_KV = WS_PQ + (size_t)2048 * 1024 * 2;
constexpr size_t WS_Z  = WS_KV + (size_t)8 * 32 * 1024 * 4;
constexpr size_t WS_G  = WS_Z + (size_t)8 * 32 * 32 * 4;
constexpr size_t WS_FL = WS_G + 2048 * 4;
constexpr int N_FLAG_WORDS = 64 * 137;
constexpr size_t WS_PART = WS_FL + 65536;

#define SCHED() __builtin_amdgcn_sched_barrier(0)
#define WAIT_V(n) asm volatile("s_waitcnt vmcnt(%0)" ::"n"(n) : "memory")
#define WAIT_L(n) asm volatile("s_waitcnt lgkmcnt(%0)" ::"n"(n) : "memory")
#define BAR() __builtin_amdgcn_s_barrier()

template <int OFF> DI void rd128(bf16x8& d, unsigned a) { asm volatile("ds_read_b128 %0, %1 offset:%2" : "=v"(d) : "v"(a), "i"(OFF) : "memory"); }
template <int OFF> DI void rdtr(s16x4& d, unsigned a) { asm volatile("ds_read_b64_tr_b16 %0, %1 offset:%2" : "=v"(d) : "v"(a), "i"(OFF) : "memory"); }
DI void gload1(float& d, const void* p) { asm volatile("global_load_dword %0, %1, off" : "=v"(d) : "v"(p) : "memory"); }
DI void gload2(u32x2& d, const void* p) { asm volatile("global_load_dwordx2 %0, %1, off" : "=v"(d) : "v"(p) : "memory"); }
template <class T> DI void pin(T& v) { asm volatile("" : "+v"(v)); }
template <class T> DI void keep(const T& v) { asm volatile("" :: "v"(v)); }
DI rsrc_t mk_rsrc(const void* p, unsigned n = 0x7fffffff) { return __builtin_amdgcn_make_buffer_rsrc((void*)p, 0, n, 0x00020000); }
DI void dma16(rsrc_t r, unsigned voff, unsigned soff, LAS char* l) { __builtin_amdgcn_raw_ptr_buffer_load_lds(r, (LAS void*)l, 16, voff, soff, 0, 0); }
DI unsigned cvt_pk(float lo, float hi) { unsigned r; asm("v_cvt_pk_bf16_f32 %0, %1, %2" : "=v"(r) : "v"(lo), "v"(hi)); return r; }
DI float bflo(unsigned w) { return __uint_as_float(w << 16); }
DI float bfhi(unsigned w) { return __uint_as_float(w & 0xffff0000u); }
constexpr float LOG2E = 1.44269504088896f;
DI float phi_f(float t) { const float e = __builtin_amdgcn_exp2f(__builtin_fminf(t * LOG2E, 0.f)); return e + __builtin_fmaxf(t + 1e-6f, 1e-6f); }
DI f32x2 mish2(f32x2 h) {
  const f32x2 t = h * LOG2E;
  f32x2 e; e.x = __builtin_amdgcn_exp2f(t.x); e.y = __builtin_amdgcn_exp2f(t.y);
  const f32x2 a = __builtin_elementwise_fma(e, (f32x2){0.5f, 0.5f}, (f32x2){1.f, 1.f});
  const f32x2 d = __builtin_elementwise_fma(e, a, (f32x2){1.f, 1.f});
  f32x2 r; r.x = __builtin_amdgcn_rcpf(d.x); r.y = __builtin_amdgcn_rcpf(d.y);
  return __builtin_elementwise_fma(-h, r, h);
}
DI f32x4 mish4(const f32x4& v) { const f32x2 a = mish2((f32x2){v[0], v[1]}), b = mish2((f32x2){v[2], v[3]}); return (f32x4){a.x, a.y, b.x, b.y}; }
DI f32x4 phi4(const f32x4& v) { return (f32x4){phi_f(v[0]), phi_f(v[1]), phi_f(v[2]), phi_f(v[3])}; }
#define MFMA16(a, b, c) __builtin_amdgcn_mfma_f32_16x16x32_bf16((a), (b), (c), 0, 0, 0)
#define CAT(L, H) __builtin_shufflevector((L), (H), 0, 1, 2, 3, 4, 5, 6, 7)
template <class F, int... I> DI void static_for_impl(F&& f, std::integer_sequence<int, I...>) { (f(std::integral_constant<int, I>{}), ...); }
template <int N, class F> DI void static_for(F&& f) { static_for_impl(f, std::make_integer_sequence<int, N>{}); }

constexpr int K1_XS = 32768, K1_WS = 26624, K1_WOFF = 3 * K1_XS, K1_DUMMY = K1_WOFF + 2 * K1_WS, K1_LDS = K1_DUMMY + 1024;

struct K1Args {
  const u16* x; const u16* wqkv; const u16* w1; const u16* b1; const u16* gw; char* ws; const u16* w2; const u16* wo;
};
DI void touch(const void* p, LAS char* dummy) { __builtin_amdgcn_global_load_lds((const unsigned*)p, (LAS unsigned*)dummy, 4, 0, 0); }

template <bool KV, int WN> constexpr int blk_panel(int b) { return b == 6 ? 3 : (KV ? (b >> 1) : (b < 4 ? WN : 2)); }
template <bool KV, int WN> constexpr int blk_cb(int b) { return b == 6 ? 0 : (KV ? 2 * WN + (b & 1) : (b < 4 ? b : 2 * WN + (b & 1))); }
template <bool KV, int WN> constexpr bool blk_std(int b) { return KV && b < 4; }
template <bool KV, int WN> constexpr bool blk_bias(int b) { return KV ? b >= 4 : (WN == 1 || b >= 4); }

template <bool KV, int WN> DI void k1_epilogue(const K1Args& A, LAS char* lds, f32x4 (&acc)[4][7], int panel, int hp, int wm);
template <bool KV, int WN>
DI void k1_body(const K1Args& A, LAS char* lds, int panel, int hp, int j32, int wm, int lane) {
  constexpr int NB = 7;
  const int wid = WN * 4 + wm;
#define STAMP_AT(i)
  const int G = hp >> 1, a = hp & 1;
  const int g4 = lane >> 4, l15 = lane & 15;
  const int c0 = (KV ? 1024 : 0) + 64 * hp;
  const int c1 = KV ? (2048 + 64 * hp) : (448 * G + 128 + 128 * a);
  const int c2 = KV ? (448 * G + 64 * a) : (448 * G + 128 + 128 * a + 64);
  const int c3 = 448 * G + 384 + (KV ? 0 : 32) + 16 * a;
  auto pcol = [&](int P) { return P == 0 ? c0 : P == 1 ? c1 : P == 2 ? c2 : c3; };
  f32x4 acc[4][NB];
  static_for<NB>([&](auto B) {
    constexpr int b = B;
    f32x4 init = {0.f, 0.f, 0.f, 0.f};
    if constexpr (blk_bias<KV, WN>(b)) {
      const u32x2 w = *(const u32x2*)((const char*)A.b1 + (pcol(blk_panel<KV, WN>(b)) + 16 * blk_cb<KV, WN>(b) + 4 * g4) * 2);
      init = (f32x4){bflo(w.x), bfhi(w.x), bflo(w.y), bfhi(w.y)};
    }
#pragma unroll
    for (int mb = 0; mb < 4; ++mb) acc[mb][b] = init;
  });
  const unsigned ldsb = (unsigned)(size_t)lds;
  unsigned xcur = ldsb + (64 * wm + l15) * 128 + ((g4 ^ ((lane >> 1) & 7)) * 16);
  const int q4 = l15 >> 2, p4 = lane & 3;
  const int sr = ((q4 >> 1) & 1) | ((g4 & 1) << 1);
  unsigned aw[4];
#pragma unroll
  for (int c = 0; c < 4; ++c) aw[c] = ldsb + K1_WOFF + (8 * g4 + q4) * 128 + ((c ^ sr) * 32) + p4 * 8;
  unsigned an[2];
#pragma unroll
  for (int t = 0; t < 2; ++t) an[t] = ldsb + K1_WOFF + 24576 + (8 * g4 + ((4 * t) ^ (4 * (g4 & 1))) + q4) * 32 + p4 * 8;

  bf16x8 Af[4]; s16x4 Bl[NB], Bh[NB];
  auto readA = [&](auto MB, unsigned addr) { rd128<MB * 2048>(Af[MB], addr); };
  auto readBh = [&](auto WP, auto KS, auto B, auto H) {
    constexpr int b = B, P = blk_panel<KV, WN>(b), c = blk_cb<KV, WN>(b), wo = WP * K1_WS;
    if constexpr (P < 3) { if constexpr (H == 0) rdtr<wo + P * 8192 + KS * 4096>(Bl[b], aw[c]); else rdtr<wo + P * 8192 + KS * 4096 + 512>(Bh[b], aw[c]); }
    else { if constexpr (H == 0) rdtr<wo + KS * 1024>(Bl[b], an[0]); else rdtr<wo + KS * 1024>(Bh[b], an[1]); }
  };
  auto mma = [&](auto B, auto MB) {
    constexpr int b = B, mb = MB;
    if constexpr (blk_std<KV, WN>(b)) acc[mb][b] = MFMA16(Af[mb], CAT(Bl[b], Bh[b]), acc[mb][b]);
    else acc[mb][b] = MFMA16(CAT(Bl[b], Bh[b]), Af[mb], acc[mb][b]);
  };
  using I0 = std::integral_constant<int, 0>; using I1 = std::integral_constant<int, 1>;
  using I2 = std::integral_constant<int, 2>; using I3 = std::integral_constant<int, 3>;
  using I5 = std::integral_constant<int, 5>; using I6 = std::integral_constant<int, 6>;
  auto h1_reads = [&](auto U, auto WP, auto KS, unsigned xa_) {
    constexpr int u = U;
    if constexpr (WN == 0) {
      if constexpr (u == 0) { readA(I0{}, xa_); readBh(WP, KS, I6{}, I0{}); }
      else if constexpr (u == 1) { readA(I1{}, xa_); readBh(WP, KS, I6{}, I1{}); }
      else readBh(WP, KS, std::integral_constant<int, ((u - 2) >> 1)>{}, std::integral_constant<int, (u & 1)>{});
    } else {
      if constexpr (u == 0) readA(I0{}, xa_);
      else if constexpr (u == 1) readA(I1{}, xa_);
      else if constexpr (u < 4) readBh(WP, KS, I6{}, std::integral_constant<int, (u & 1)>{});
      else readBh(WP, KS, std::integral_constant<int, ((u - 4) >> 1)>{}, std::integral_constant<int, (u & 1)>{});
    }
  };
  constexpr int L0 = WN == 0 ? 14 : 12, L1 = 26 - L0;
  auto step = [&](auto WP, auto KS, unsigned xcur, auto WPN, auto KSN, unsigned xnext, auto BARRIER) {
    static_for<L0>([&](auto S) {
      constexpr int s = S;
      if constexpr (s == 0 || s == 2 || s == 4) WAIT_L(8);
      if constexpr (s == 6) WAIT_L(6);
      if constexpr (s == 8) WAIT_L(4);
      if constexpr (s == 10) WAIT_L(2);
      if constexpr ((s & 1) == 0) SCHED();
      if constexpr (s < 12) mma(std::integral_constant<int, (s >> 1)>{}, std::integral_constant<int, (s & 1)>{}); else mma(I6{}, std::integral_constant<int, (s & 1)>{});
      if constexpr (s == 0) readBh(WP, KS, I5{}, I0{});
      if constexpr (s == 1) readBh(WP, KS, I5{}, I1{});
      if constexpr (s == 2) readA(I2{}, xcur);
      if constexpr (s == 3) readA(I3{}, xcur);
      SCHED();
    });
    WAIT_L(0);
    if constexpr (decltype(BARRIER)::value) {
      BAR();
      if constexpr (WN == 0) __builtin_amdgcn_s_setprio(2);
    }
    else { if constexpr (WN == 0) __builtin_amdgcn_s_setprio(0); }
    SCHED();
    static_for<L1>([&](auto U) {
      constexpr int u = U;
      if constexpr (WN == 0) mma(std::integral_constant<int, (u >> 1)>{}, std::integral_constant<int, 2 + (u & 1)>{});
      else { if constexpr (u < 2) mma(I6{}, std::integral_constant<int, 2 + u>{}); else mma(std::integral_constant<int, ((u - 2) >> 1)>{}, std::integral_constant<int, 2 + (u & 1)>{}); }
      h1_reads(U, WPN, KSN, xnext);
      SCHED();
    });
  };
  STAMP_AT(1);
  BAR(); SCHED();
  STAMP_AT(2);
  static_for<L1>([&](auto U) { h1_reads(U, I0{}, I0{}, xcur); });
  if constexpr (WN == 0) __builtin_amdgcn_s_setprio(2); else __builtin_amdgcn_s_setprio(1);
  int xs = 0;
  auto ktile = [&](auto WP, int t) {
    unsigned x1 = xcur ^ 64u, xn = xcur + (xs == 2 ? (unsigned)(-2 * K1_XS) : (unsigned)K1_XS);
    pin(x1); pin(xn);
    step(WP, I0{}, xcur, WP, I1{}, x1, std::false_type{});
    step(WP, I1{}, x1, std::integral_constant<int, WP ^ 1>{}, I0{}, xn, std::true_type{});
    xcur = xn; xs = xs == 2 ? 0 : xs + 1;
  };
#pragma unroll 1
  for (int t = 0; t < 16; t += 2) { ktile(I0{}, t); ktile(I1{}, t + 1); }
  SCHED(); WAIT_L(0); SCHED();
  __builtin_amdgcn_s_setprio(0);
  STAMP_AT(3);

  k1_epilogue<KV, WN>(A, lds, acc, panel, hp, wm);
}

template <bool KV, int WN>
DI void k1_epilogue(const K1Args& A, LAS char* lds, f32x4 (&acc)[4][7], int panel, int hp, int wm) {
  int lane = threadIdx.x & 63; asm volatile("" : "+v"(lane));
  const int g4 = lane >> 4, l15 = lane & 15;
  const int G = hp >> 1, a = hp & 1;
  const int c0 = (KV ? 1024 : 0) + 64 * hp;
  const int c1 = KV ? (2048 + 64 * hp) : (448 * G + 128 + 128 * a);
  const int c2 = KV ? (448 * G + 64 * a) : (448 * G + 128 + 128 * a + 64);
  const int c3 = 448 * G + 384 + (KV ? 0 : 32) + 16 * a;
  using I0 = std::integral_constant<int, 0>; using I1 = std::integral_constant<int, 1>;
  u16* xa = (u16*)(A.ws + WS_XA);
  const size_t rowbase = (size_t)panel * 256 + 64 * wm;
  LAS char* stg = lds + (WN * 4 + wm) * 8192;
  const int chunk_g = (g4 & 1) * 2 + (g4 >> 1);
  auto store_line = [&](auto B0, auto ACT, u16* base, size_t ld, int col0) {
    static_for<4>([&](auto MB) {
      constexpr int mb = MB;
      static_for<2>([&](auto PP) {
        constexpr int p = PP;
        f32x4 e = acc[mb][B0 + 2 * p], o = acc[mb][B0 + 2 * p + 1];
        if constexpr (ACT == 0) { e = phi4(e); o = phi4(o); } else { e = mish4(e); o = mish4(o); }
        const unsigned e0 = cvt_pk(e[0], e[1]), e1 = cvt_pk(e[2], e[3]), o0 = cvt_pk(o[0], o[1]), o1 = cvt_pk(o[2], o[3]);
        const auto r0 = __builtin_amdgcn_permlane16_swap(e0, o0, false, false);
        const auto r1 = __builtin_amdgcn_permlane16_swap(e1, o1, false, false);
        const u32x4 w = {r0[0], r1[0], r0[1], r1[1]};
        *(LAS u32x4*)(stg + (16 * mb + l15) * 128 + (((4 * p + chunk_g) ^ (l15 & 7)) * 16)) = w;
      });
    });
#pragma unroll
    for (int i = 0; i < 8; ++i) {
      const u32x4 w = *(LAS u32x4*)(stg + i * 1024 + lane * 16);
      const int r = 8 * i + (lane >> 3), ch = (lane & 7) ^ (r & 7);
      __builtin_amdgcn_raw_buffer_store_b128(w, mk_rsrc(base), (unsigned)(((rowbase + r) * ld + col0 + ch * 8) * 2), 0, K1_AUX);
    }
  };
  auto store_half = [&](int col0) {
#pragma unroll
    for (int mb = 0; mb < 4; ++mb) {
      const f32x4 e = mish4(acc[mb][4]), o = mish4(acc[mb][5]);
      const unsigned e0 = cvt_pk(e[0], e[1]), e1 = cvt_pk(e[2], e[3]), o0 = cvt_pk(o[0], o[1]), o1 = cvt_pk(o[2], o[3]);
      const auto r0 = __builtin_amdgcn_permlane16_swap(e0, o0, false, false);
      const auto r1 = __builtin_amdgcn_permlane16_swap(e1, o1, false, false);
      const u32x4 w = {r0[0], r1[0], r0[1], r1[1]};
      __builtin_amdgcn_raw_buffer_store_b128(w, mk_rsrc(xa), (unsigned)(((rowbase + 16 * mb + l15) * XA_LD + col0 + chunk_g * 8) * 2), 0, K1_AUX);
    }
  };
  auto store_narrow = [&]() {
#pragma unroll
    for (int m2 = 0; m2 < 2; ++m2) {
      const int mb = 2 * WN + m2;
      const f32x4 v = mish4(acc[2 * WN + m2][6]);
      const u32x2 w = {cvt_pk(v[0], v[1]), cvt_pk(v[2], v[3])};
      __builtin_amdgcn_raw_buffer_store_b64(w, mk_rsrc(xa), (unsigned)(((rowbase + 16 * mb + l15) * XA_LD + c3 + 4 * g4) * 2), 0, K1_AUX);
    }
  };
  store_half(c2 + 32 * WN);
  store_narrow();
  if constexpr (!KV) {
    if constexpr (WN == 0) store_line(I0{}, I0{}, (u16*)(A.ws + WS_PQ), 1024, c0);
    else store_line(I0{}, I1{}, xa, XA_LD, c1);
  } else {
    f32x4 kvD[4]; f32x4 zD[2];
#pragma unroll
    for (int i = 0; i < 4; ++i) kvD[i] = (f32x4){0.f, 0.f, 0.f, 0.f};
    zD[0] = zD[1] = (f32x4){0.f, 0.f, 0.f, 0.f};
    const bf16x8 ones = {0x3f80, 0x3f80, 0x3f80, 0x3f80, 0x3f80, 0x3f80, 0x3f80, 0x3f80};
#pragma unroll
    for (int mp = 0; mp < 2; ++mp) {
      bf16x8 ka[2], vb[2];
#pragma unroll
      for (int b = 0; b < 2; ++b) {
        const f32x4 k0 = phi4(acc[2 * mp][b]), k1 = phi4(acc[2 * mp + 1][b]);
        const u32x4 w = {cvt_pk(k0[0], k0[1]), cvt_pk(k0[2], k0[3]), cvt_pk(k1[0], k1[1]), cvt_pk(k1[2], k1[3])};
        ka[b] = __builtin_bit_cast(bf16x8, w);
        const f32x4 v0 = acc[2 * mp][2 + b], v1 = acc[2 * mp + 1][2 + b];
        const u32x4 u = {cvt_pk(v0[0], v0[1]), cvt_pk(v0[2], v0[3]), cvt_pk(v1[0], v1[1]), cvt_pk(v1[2], v1[3])};
        vb[b] = __builtin_bit_cast(bf16x8, u);
      }
#pragma unroll
      for (int db = 0; db < 2; ++db) {
#pragma unroll
        for (int eb = 0; eb < 2; ++eb) kvD[db * 2 + eb] = MFMA16(ka[db], vb[eb], kvD[db * 2 + eb]);
        zD[db] = MFMA16(ka[db], ones, zD[db]);
      }
    }
    LAS char* sc = lds + 2 * K1_XS;
    LAS char* sz = lds + K1_WOFF;
#pragma unroll
    for (int i = 0; i < 4; ++i) *(LAS f32x4*)(sc + ((WN * 4 + wm) * 4 + i) * 1024 + lane * 16) = kvD[i];
    if (l15 == 0) {
#pragma unroll
      for (int db = 0; db < 2; ++db) *(LAS f32x4*)(sz + ((WN * 4 + wm) * 2 + db) * 64 + g4 * 16) = zD[db];
    }
    WAIT_L(0); BAR(); SCHED();
    const int blk = wm;
    f32x4 s = {0.f, 0.f, 0.f, 0.f};
#pragma unroll
    for (int m = 0; m < 4; ++m) s += *(LAS f32x4*)(sc + ((WN * 4 + m) * 4 + blk) * 1024 + lane * 16);
    float* kvp = (float*)(A.ws + WS_KV) + ((size_t)panel * 32 + 2 * hp + WN) * 1024;
    *(f32x4*)(kvp + (16 * (blk & 1) + l15) * 32 + 16 * (blk >> 1) + 4 * g4) = s;
    if (wm == 0 && lane < 8) {
      const int db = lane >> 2, gq = lane & 3;
      f32x4 zs = {0.f, 0.f, 0.f, 0.f};
#pragma unroll
      for (int m = 0; m < 4; ++m) zs += *(LAS f32x4*)(sz + ((WN * 4 + m) * 2 + db) * 64 + gq * 16);
      *(f32x4*)((float*)(A.ws + WS_Z) + ((size_t)panel * 32 + 2 * hp + WN) * 32 + 16 * db + 4 * gq) = zs;
    }
  }
}

DI void k1_loader(const K1Args& A, LAS char* lds, bool kv, int panel, int hp, int j32, bool isx, int lane) {
  const int G = hp >> 1, a = hp & 1;
  const int c0 = (kv ? 1024 : 0) + 64 * hp;
  const int c1 = kv ? (2048 + 64 * hp) : (448 * G + 128 + 128 * a);
  const int c2 = kv ? (448 * G + 64 * a) : (448 * G + 128 + 128 * a + 64);
  const int c3 = 448 * G + 384 + (kv ? 0 : 32) + 16 * a;
  unsigned vA[4], vB[4], vN[2];
#pragma unroll
  for (int k = 0; k < 4; ++k) {
    const int par = k & 1;
    const unsigned chx = (unsigned)(((lane & 7) ^ ((4 * par + (lane >> 4)) & 7)) * 16), chw = (unsigned)(((lane & 7) ^ ((((lane >> 4) & 1) | (par << 1)) << 1)) * 16);
    vA[k] = (isx ? (unsigned)((8 * k + (lane >> 3)) * 2048) + chx : (unsigned)((8 * k + (lane >> 3)) * 6144) + chw) + 3072 - k * 1024;
    vB[k] = (unsigned)((8 * k + (lane >> 3)) * 7168) + chw + 3072 - k * 1024;
  }
#pragma unroll
  for (int n = 0; n < 2; ++n) { const int rho = 32 * n + (lane >> 1); vN[n] = (unsigned)((rho ^ (((rho >> 3) & 1) << 2)) * 7168 + (lane & 1) * 16) + 3072 - n * 1024; }
  const rsrc_t rx = mk_rsrc((const char*)A.x - 3072), rq = mk_rsrc((const char*)A.wqkv - 3072), r1 = mk_rsrc((const char*)A.w1 - 3072);
  const unsigned xrow0 = (unsigned)panel * 256 * 2048;
  auto issueX = [&](int t, int xs) {
#pragma unroll
    for (int g = 0; g < 8; ++g)
      static_for<4>([&](auto K) { __builtin_amdgcn_raw_ptr_buffer_load_lds(rx, (LAS void*)(lds + xs * K1_XS + g * 4096), 16, vA[K], xrow0 + t * 128 + g * 65536, K * 1024, 0); });
  };
  auto issueW = [&](int t) {
    LAS char* dst = lds + K1_WOFF + (t & 1) * K1_WS;
#pragma unroll
    for (int g = 0; g < 2; ++g)
      static_for<4>([&](auto K) { __builtin_amdgcn_raw_ptr_buffer_load_lds(rq, (LAS void*)(dst + g * 4096), 16, vA[K], (t * 64 + g * 32) * 6144 + c0 * 2, K * 1024, 0); });
    if (kv) {
#pragma unroll
      for (int g = 0; g < 2; ++g)
        static_for<4>([&](auto K) { __builtin_amdgcn_raw_ptr_buffer_load_lds(rq, (LAS void*)(dst + 8192 + g * 4096), 16, vA[K], (t * 64 + g * 32) * 6144 + c1 * 2, K * 1024, 0); });
    } else {
#pragma unroll
      for (int g = 0; g < 2; ++g)
        static_for<4>([&](auto K) { __builtin_amdgcn_raw_ptr_buffer_load_lds(r1, (LAS void*)(dst + 8192 + g * 4096), 16, vB[K], (t * 64 + g * 32) * 7168 + c1 * 2, K * 1024, 0); });
    }
#pragma unroll
    for (int g = 0; g < 2; ++g)
      static_for<4>([&](auto K) { __builtin_amdgcn_raw_ptr_buffer_load_lds(r1, (LAS void*)(dst + 16384 + g * 4096), 16, vB[K], (t * 64 + g * 32) * 7168 + c2 * 2, K * 1024, 0); });
    static_for<2>([&](auto N) { __builtin_amdgcn_raw_ptr_buffer_load_lds(r1, (LAS void*)(dst + 24576), 16, vN[N], t * 64 * 7168 + c3 * 2, N * 1024, 0); });
  };
  unsigned tdummy = 0;
  auto touch3 = [&]() {
    const int w = blockIdx.x * 2 + (isx ? 0 : 1);
#pragma unroll
    for (int n = 0; n < 3; ++n) {
      const int k = n * 64 + lane, li = w * 144 + (k < 144 ? k : 143);
      const char* p = li < 57344 ? (const char*)A.w2 + (size_t)li * 128 : (const char*)A.wo + (size_t)(li - 57344) * 128;
      asm volatile("global_load_dword %0, %1, off" : "+v"(tdummy) : "v"(p) : "memory");
    }
  };
  if (isx) {
    issueX(0, 0); issueX(1, 1);
#pragma unroll
    for (int n = 0; n < 4; ++n)
      asm volatile("global_load_dword %0, %1, off" : "+v"(tdummy) : "v"((const char*)A.x + (size_t)(panel * 256 + 16 * (j32 & 15) + 4 * n + (lane >> 4)) * 2048 + (lane & 15) * 128) : "memory");
    WAIT_V(36); BAR();
    issueX(2, 2);
    int xs = 0;
#pragma unroll 1
    for (int t = 0; t < 13; ++t) {
      WAIT_V(32); BAR(); issueX(t + 3, xs); xs = xs == 2 ? 0 : xs + 1;
      if (t < 7) {
        const int li = blockIdx.x * 52 + (lane < 52 ? lane : 51);
        int row = 64 * (2 * t + 3) + li / 104; row = row < 1024 ? row : 1023;
        const int c = li % 104;
        const char* p = c < 48 ? (const char*)A.wqkv + (size_t)row * 6144 + c * 128 : (const char*)A.w1 + (size_t)row * 7168 + (c - 48) * 128;
        asm volatile("global_load_dword %0, %1, off" : "+v"(tdummy) : "v"(p) : "memory");
      }
    }
    touch3();
    WAIT_V(35); BAR();
    WAIT_V(3); BAR();
    BAR();
  } else {
    issueW(0); issueW(1);
    WAIT_V(26); BAR();
#pragma unroll 1
    for (int t = 0; t < 14; ++t) { WAIT_V(0); BAR(); issueW(t + 2); }
    touch3();
    WAIT_V(3); BAR();
    BAR();
  }
#pragma unroll 1
  for (int r = 0; r < 4; ++r) {
    const int row = panel * 256 + j32 * 8 + (isx ? 0 : 4) + r;
    const u32x4* xr = (const u32x4*)((const char*)A.x + (size_t)row * 2048 + lane * 32);
    const u32x4* gr = (const u32x4*)((const char*)A.gw + lane * 32);
    const u32x4 gx0 = xr[0], gx1 = xr[1], gg0 = gr[0], gg1 = gr[1];
    float s = 0.f;
#pragma unroll
    for (int i = 0; i < 4; ++i) {
      s = __builtin_fmaf(bflo(gx0[i]), bflo(gg0[i]), s); s = __builtin_fmaf(bfhi(gx0[i]), bfhi(gg0[i]), s);
      s = __builtin_fmaf(bflo(gx1[i]), bflo(gg1[i]), s); s = __builtin_fmaf(bfhi(gx1[i]), bfhi(gg1[i]), s);
    }
#pragma unroll
    for (int o = 32; o >= 1; o >>= 1) s += __shfl_xor(s, o);
    const float gval = __builtin_amdgcn_rcpf(1.f + __builtin_amdgcn_exp2f(-LOG2E * s));
    if (lane == 0) ((float*)(A.ws + WS_G))[row] = gval;
  }
  if (kv) BAR();
  WAIT_V(0); keep(tdummy);
}

__global__ __launch_bounds__(640, 3) void k1_kernel(K1Args A) {
  extern __shared__ __attribute__((aligned(16))) char smem[];
  LAS char* lds = (LAS char*)smem;
  const int b = blockIdx.x, xcd = b & 7, idx = b >> 3, rq = xcd >> 1, ch = xcd & 1;
  const int panel = 2 * rq + (idx >> 4), c16 = idx & 15, hp = 8 * ch + (c16 >> 1), j32 = ch * 16 + c16;
  const int wid = __builtin_amdgcn_readfirstlane(threadIdx.x >> 6), lane = threadIdx.x & 63;
  if (b == 0 && threadIdx.x < 137) ((unsigned*)(A.ws + WS_FL))[threadIdx.x * 64] = 0u;
  if (wid >= 8) { k1_loader(A, lds, (c16 & 1) != 0, panel, hp, j32, wid == 8, lane); return; }
  if (c16 & 1) {
    if (wid < 4) k1_body<true, 0>(A, lds, panel, hp, j32, wid, lane); else k1_body<true, 1>(A, lds, panel, hp, j32, wid - 4, lane);
  } else {
    if (wid < 4) k1_body<false, 0>(A, lds, panel, hp, j32, wid, lane); else k1_body<false, 1>(A, lds, panel, hp, j32, wid - 4, lane);
  }
}

#define K2_NA 26
constexpr int K2_ST = 32768, K2_SCR = 4 * K2_ST, K2_LDS = 4 * K2_ST + 4096;
struct K2Args { const u16* w2; const u16* wo; char* ws; u16* out; };
typedef __attribute__((address_space(1))) unsigned gu32;
#define RLX_AGENT __ATOMIC_RELAXED, __HIP_MEMORY_SCOPE_AGENT
DI void dma16_sc1(rsrc_t r, unsigned voff, unsigned soff, LAS char* l) { __builtin_amdgcn_raw_ptr_buffer_load_lds(r, (LAS void*)l, 16, voff, soff, 0, 16); }
DI void poll_issue(unsigned& d, const gu32* p) { asm volatile("global_load_dword %0, %1, off sc1" : "=v"(d) : "v"(p) : "memory"); }
DI bool wait_ge(gu32* word, unsigned want, gu32* tmo) {
  for (unsigned spins = 0;; ++spins) {
    if ((unsigned)__builtin_amdgcn_readfirstlane(__hip_atomic_load(word, RLX_AGENT)) >= want) return true;
    if (spins > (1u << 20)) { __hip_atomic_store(tmo, 1u, RLX_AGENT); return false; }
    __builtin_amdgcn_s_sleep(2);
  }
}

__global__ __launch_bounds__(640, 3) void k2_kernel(K2Args A) {
  extern __shared__ __attribute__((aligned(16))) char smem[];
  LAS char* lds = (LAS char*)smem;
  const int b = blockIdx.x, xcd = b & 7, idx = b >> 3, rq = xcd >> 1, role = xcd & 1;
  const int tm = 4 * rq + (idx >> 3), tn = idx & 7;
  const int wid = __builtin_amdgcn_readfirstlane(threadIdx.x >> 6), lane = threadIdx.x & 63;
  gu32* flags = (gu32*)(A.ws + WS_FL);
  const int tile = tm * 8 + tn;
  const int pp_ = 2 * rq + (idx >> 4), hh = 16 * role + (idx & 15);
  const int nt = role == 0 ? K2_NA + 16 : 56 - K2_NA;
  if (wid >= 8) {
    const bool isx = wid == 8;
    unsigned voff[4];
#pragma unroll
    for (int k = 0; k < 4; ++k) {
      const int par = k & 1;
      const int swx = (4 * par + (lane >> 4)) & 7, sww = (((lane >> 4) & 1) | (par << 1)) << 1;
      voff[k] = (isx ? (unsigned)((8 * k + (lane >> 3)) * (XA_LD * 2) + (((lane & 7) ^ swx) * 16)) : (unsigned)((8 * k + (lane >> 3)) * 2048 + (((lane & 7) ^ sww) * 16))) + 3072 - k * 1024;
    }
    const unsigned gstep = isx ? 32 * XA_LD * 2 : 32 * 2048;
    auto issue = [&](rsrc_t r, unsigned so, int stage) {
      LAS char* st = lds + stage * K2_ST + (isx ? 0 : 16384);
#pragma unroll
      for (int g = 0; g < 4; ++g) {
        const unsigned sg = isx ? so + g * gstep : so + (g & 1) * gstep + (g >> 1) * 128;
        static_for<4>([&](auto K) { __builtin_amdgcn_raw_ptr_buffer_load_lds(r, (LAS void*)(st + g * 4096), 16, voff[K], sg, K * 1024, 16); });
      }
    };
    const rsrc_t r0 = mk_rsrc(isx ? (const char*)(A.ws + WS_XA) - 3072 : (const char*)A.w2 - 3072);
    const rsrc_t r1 = mk_rsrc(isx ? (const char*)(A.ws + WS_XA) - 3072 : (const char*)A.wo - 3072);
    const unsigned tstep = isx ? 128u : 64u * 2048u;
    const unsigned sbase = isx ? (unsigned)tm * 128 * (XA_LD * 2) : (unsigned)tn * 256;
    const unsigned s0 = sbase + (role == 0 ? 0 : K2_NA) * tstep;
    const unsigned s1 = isx ? sbase + 56 * tstep : sbase;
    const rsrc_t rp = mk_rsrc(A.ws + WS_PART);
    auto issue_partial = [&]() {
#pragma unroll
      for (int half = 0; half < 2; ++half)
#pragma unroll
        for (int g = 0; g < 4; ++g)
          static_for<4>([&](auto K) { __builtin_amdgcn_raw_ptr_buffer_load_lds(rp, (LAS void*)(lds + ((nt + half) & 3) * K2_ST + (isx ? 0 : 16384) + g * 4096), 16, lane * 16,
                                                                                (unsigned)tile * 65536 + half * 32768 + (isx ? 0 : 16384) + g * 4096, K * 1024, 16); });
    };
    issue(r0, s0, 0); issue(r0, s0 + tstep, 1); issue(r0, s0 + 2 * tstep, 2);
    WAIT_V(32); BAR();
    issue(r0, s0 + 3 * tstep, 3);
    auto run = [&](int i0, int i1, rsrc_t r, unsigned so) {
#pragma unroll 1
      for (int i = i0; i < i1; ++i, so += tstep) { WAIT_V(32); BAR(); issue(r, so, i & 3); }
    };
    const int nA = role == 0 ? K2_NA : nt;
    unsigned pv = 0;
    run(0, 5, r0, s0 + 4 * tstep);
    if (isx && lane == 0) __hip_atomic_fetch_add(flags + 64 * pp_, 1u, RLX_AGENT);
    if (role == 0) {
      run(5, K2_NA - 8, r0, s0 + 9 * tstep);
      if (isx) poll_issue(pv, flags + 64 * (tm >> 1));
      run(K2_NA - 8, K2_NA - 4, r0, s0 + (K2_NA - 4) * tstep);
      if (isx) {
        WAIT_V(32); pin(pv);
        if ((unsigned)__builtin_amdgcn_readfirstlane(pv) < 32u) wait_ge(flags + 64 * (tm >> 1), 32u, flags + 64 * 136);
      }
      run(K2_NA - 4, nt - 4, r1, s1);
    } else run(5, nt - 4, r0, s0 + 9 * tstep);
    poll_issue(pv, flags + 64 * (8 + tile));
    WAIT_V(33); BAR();
    WAIT_V(17); BAR();
    WAIT_V(0);
    bool early = false;
    if (role == 0) { pin(pv); early = (unsigned)__builtin_amdgcn_readfirstlane(pv) != 0u; }
    BAR();
    if (early) issue_partial();
    BAR();
    (void)nA;
    if (role == 0) {
      if (!early) wait_ge(flags + 64 * (8 + tile), 1u, flags + 64 * 136);
      BAR();
      if (!early) issue_partial();
      WAIT_V(0);
      BAR();
    } else {
      BAR();
      if (isx && lane == 0) __hip_atomic_store(flags + 64 * (8 + tile), 1u, RLX_AGENT);
    }
    return;
  }
  const int wm = wid >> 2, wn = wid & 3;
  const int g4 = lane >> 4, l15 = lane & 15;
  const size_t prow = (size_t)pp_ * 256 + 32 * wid;
  f32x2 part[8]; bf16x8 pf[2]; float gv[2], gq[4]; f32x4 zq;
  {
    const float* kvp = (const float*)(A.ws + WS_KV) + (size_t)hh * 1024 + (wid * 64 + lane) * 2;
#pragma unroll
    for (int q = 0; q < 8; ++q) part[q] = *(const f32x2*)(kvp + (size_t)q * 32 * 1024);
    zq = *(const f32x4*)((const float*)(A.ws + WS_Z) + (size_t)(lane >> 3) * 1024 + hh * 32 + (lane & 7) * 4);
#pragma unroll
    for (int mb = 0; mb < 2; ++mb) {
      pf[mb] = *(const bf16x8*)((const u16*)(A.ws + WS_PQ) + (prow + 16 * mb + l15) * 1024 + 32 * hh + 8 * g4);
      gv[mb] = ((const float*)(A.ws + WS_G))[prow + 16 * mb + l15];
    }
#pragma unroll
    for (int mb = 0; mb < 4; ++mb) gq[mb] = ((const float*)(A.ws + WS_G))[tm * 128 + 64 * wm + 16 * mb + l15];
  }
  const unsigned ldsb = (unsigned)(size_t)lds;
  unsigned ax[2];
#pragma unroll
  for (int ks = 0; ks < 2; ++ks) ax[ks] = ldsb + (64 * wm + l15) * 128 + (((4 * ks + g4) ^ ((lane >> 1) & 7)) * 16);
  const int q4 = l15 >> 2, p4 = lane & 3;
  const int sr = ((q4 >> 1) & 1) | ((g4 & 1) << 1);
  unsigned aw[2];
#pragma unroll
  for (int nb = 0; nb < 2; ++nb) aw[nb] = ldsb + 16384 + (wn >> 1) * 8192 + (8 * g4 + q4) * 128 + (((2 * (wn & 1) + nb) ^ sr) * 32) + p4 * 8;
  f32x4 acc[4][2];
#pragma unroll
  for (int i = 0; i < 4; ++i) acc[i][0] = acc[i][1] = (f32x4){0.f, 0.f, 0.f, 0.f};
  bf16x8 Af[2][2][4]; s16x4 Bl[2][2][2], Bh[2][2][2];
  auto rd = [&](auto BUF, auto I, const unsigned (&pa)[2], const unsigned (&pw)[2]) {
    constexpr int i = I, ks = i >> 3, r = i & 7;
    if constexpr (r < 2) { if constexpr (r == 0) rdtr<ks * 4096>(Bl[BUF][ks][0], pw[0]); else rdtr<ks * 4096 + 512>(Bh[BUF][ks][0], pw[0]); }
    else if constexpr (r < 6) rd128<(r - 2) * 2048>(Af[BUF][ks][r - 2], pa[ks]);
    else { if constexpr (r == 6) rdtr<ks * 4096>(Bl[BUF][ks][1], pw[1]); else rdtr<ks * 4096 + 512>(Bh[BUF][ks][1], pw[1]); }
  };
  using I0 = std::integral_constant<int, 0>; using I1 = std::integral_constant<int, 1>;
  STAMP_AT(1);
  BAR(); SCHED();
  STAMP_AT(2);
  LAS char* kvs = lds + K2_SCR;
  {
    const f32x2 s2 = ((part[0] + part[1]) + (part[2] + part[3])) + ((part[4] + part[5]) + (part[6] + part[7]));
    *(LAS unsigned*)(kvs + (wid * 64 + lane) * 4) = cvt_pk(s2.x, s2.y);
    if (wid == 0) {
      f32x4 z = zq;
#pragma unroll
      for (int o = 8; o <= 32; o <<= 1) { z[0] += __shfl_xor(z[0], o); z[1] += __shfl_xor(z[1], o); z[2] += __shfl_xor(z[2], o); z[3] += __shfl_xor(z[3], o); }
      if (lane < 8) *(LAS u32x2*)(kvs + 2048 + lane * 8) = (u32x2){cvt_pk(z[0], z[1]), cvt_pk(z[2], z[3])};
    }
    SCHED();
  }
  static_for<16>([&](auto I) { rd(I0{}, I, ax, aw); });
  auto body = [&](auto CUR, int i) {
    constexpr int cur = CUR;
    const unsigned so1 = ((i + 1) & 3) * K2_ST;
    unsigned pa[2] = {ax[0] + so1, ax[1] + so1}, pw[2] = {aw[0] + so1, aw[1] + so1};
    pin(pa[0]); pin(pa[1]); pin(pw[0]); pin(pw[1]);
    WAIT_L(0); BAR(); SCHED();
    static_for<16>([&](auto S) {
      constexpr int s = S, ks = s >> 3, nb = (s >> 2) & 1, mb = s & 3;
      acc[mb][nb] = MFMA16(CAT(Bl[cur][ks][nb], Bh[cur][ks][nb]), Af[cur][ks][mb], acc[mb][nb]);
      if constexpr (s < 8) { rd(std::integral_constant<int, cur ^ 1>{}, S, pa, pw); }
      else if constexpr (s < 12) { rd(std::integral_constant<int, cur ^ 1>{}, std::integral_constant<int, 2 * s - 8>{}, pa, pw); rd(std::integral_constant<int, cur ^ 1>{}, std::integral_constant<int, 2 * s - 7>{}, pa, pw); }
      SCHED();
    });
  };
  body(I0{}, 0);
  {
    bf16x8 ka[2], za;
    const unsigned kb = (unsigned)(size_t)kvs;
    rd128<0>(ka[0], kb + l15 * 64 + g4 * 16); rd128<1024>(ka[1], kb + l15 * 64 + g4 * 16); rd128<2048>(za, kb + g4 * 16);
    WAIT_L(0); SCHED();
    const rsrc_t ro = mk_rsrc(A.ws + WS_XA);
    const int chunk_g = (g4 & 1) * 2 + (g4 >> 1);
#pragma unroll
    for (int mb = 0; mb < 2; ++mb) {
      const f32x4 zero = {0.f, 0.f, 0.f, 0.f};
      const f32x4 n0 = MFMA16(ka[0], pf[mb], zero), n1 = MFMA16(ka[1], pf[mb], zero), dn = MFMA16(za, pf[mb], zero);
      const float sc = gv[mb] * __builtin_amdgcn_rcpf(dn[0] + 1e-6f);
      const f32x4 e = n0 * sc, o = n1 * sc;
      const unsigned e0 = cvt_pk(e[0], e[1]), e1 = cvt_pk(e[2], e[3]), o0 = cvt_pk(o[0], o[1]), o1 = cvt_pk(o[2], o[3]);
      const auto r0 = __builtin_amdgcn_permlane16_swap(e0, o0, false, false);
      const auto r1 = __builtin_amdgcn_permlane16_swap(e1, o1, false, false);
      const u32x4 w = {r0[0], r1[0], r0[1], r1[1]};
      const unsigned off = (unsigned)(((prow + 16 * mb + l15) * XA_LD + 3584 + 32 * hh + chunk_g * 8) * 2);
      __builtin_amdgcn_raw_buffer_store_b128(w, ro, off, 0, 16);
    }
    SCHED();
  }
  body(I1{}, 1);
  body(I0{}, 2);
  float omg[4];
#pragma unroll
  for (int mb = 0; mb < 4; ++mb) omg[mb] = 1.f - gq[mb];
  body(I1{}, 3);
  WAIT_V(0);
  WAIT_L(0); SCHED();
  const int n1 = role == 0 ? K2_NA : nt;
  for (int i = 4; i < n1; i += 2) { body(I0{}, i); body(I1{}, i + 1); }
  SCHED(); WAIT_L(0); SCHED();
#pragma unroll
  for (int mb = 0; mb < 4; ++mb) { acc[mb][0] *= omg[mb]; acc[mb][1] *= omg[mb]; }
  for (int i = n1; i < nt; i += 2) { body(I0{}, i); body(I1{}, i + 1); }
  SCHED(); WAIT_L(0); SCHED();
  STAMP_AT(3);
  if (role == 1) {
    const rsrc_t rp = mk_rsrc(A.ws + WS_PART);
    const unsigned poff = (unsigned)((tile * 8 + wid) * 8192 + lane * 16);
#pragma unroll
    for (int mb = 0; mb < 4; ++mb)
#pragma unroll
      for (int nb = 0; nb < 2; ++nb) __builtin_amdgcn_raw_buffer_store_b128(__builtin_bit_cast(u32x4, acc[mb][nb]), rp, poff + (mb * 2 + nb) * 1024, 0, 16);
    STAMP_AT(4);
    WAIT_V(0);
    BAR();
  } else {
    BAR();
    BAR();
    STAMP_AT(4);
    const int chunk_g = (g4 & 1) * 2 + (g4 >> 1);
    LAS char* pl = lds + ((nt + (wid >> 2)) & 3) * K2_ST + (wid & 3) * 8192 + lane * 16;
#pragma unroll
    for (int mb = 0; mb < 4; ++mb) {
      const f32x4 e = acc[mb][0] + *(LAS f32x4*)(pl + (mb * 2) * 1024), o = acc[mb][1] + *(LAS f32x4*)(pl + (mb * 2 + 1) * 1024);
      const unsigned e0 = cvt_pk(e[0], e[1]), e1 = cvt_pk(e[2], e[3]), o0 = cvt_pk(o[0], o[1]), o1 = cvt_pk(o[2], o[3]);
      const auto r0 = __builtin_amdgcn_permlane16_swap(e0, o0, false, false);
      const auto r1 = __builtin_amdgcn_permlane16_swap(e1, o1, false, false);
      const u32x4 w = {r0[0], r1[0], r0[1], r1[1]};
      *(u32x4*)(A.out + (size_t)(tm * 128 + 64 * wm + 16 * mb + l15) * 1024 + tn * 128 + 32 * wn + chunk_g * 8) = w;
    }
  }
}

extern "C" void kernel_launch(void* const* d_in, const int* in_sizes, int n_in, void* d_out, int out_size, void* d_ws, size_t ws_size, hipStream_t stream) {
  K1Args a; a.x = (const u16*)d_in[0]; a.wqkv = (const u16*)d_in[1]; a.w1 = (const u16*)d_in[4]; a.b1 = (const u16*)d_in[5]; a.gw = (const u16*)d_in[7]; a.ws = (char*)d_ws; a.w2 = (const u16*)d_in[6]; a.wo = (const u16*)d_in[2];
  (void)hipFuncSetAttribute((const void*)k1_kernel, hipFuncAttributeMaxDynamicSharedMemorySize, K1_LDS);
  (void)hipFuncSetAttribute((const void*)k2_kernel, hipFuncAttributeMaxDynamicSharedMemorySize, K2_LDS);
  k1_kernel<<<256, 640, K1_LDS, stream>>>(a);
  K2Args c; c.w2 = (const u16*)d_in[6]; c.wo = (const u16*)d_in[2]; c.ws = (char*)d_ws; c.out = (u16*)d_out;
  k2_kernel<<<256, 640, K2_LDS, stream>>>(c);
}
```

```cpp
#include <hip/hip_runtime.h>
#include <stdint.h>
#include <utility>
#define ALT 3
#define LAS __attribute__((address_space(3)))
#define DI __device__ __forceinline__
typedef unsigned short u16;
typedef short bf16x8 __attribute__((ext_vector_type(8)));
typedef short s16x4 __attribute__((ext_vector_type(4)));
typedef float f32x4 __attribute__((ext_vector_type(4)));
typedef float f32x2 __attribute__((ext_vector_type(2)));
typedef unsigned u32x4 __attribute__((ext_vector_type(4)));
typedef unsigned u32x2 __attribute__((ext_vector_type(2)));
typedef __amdgpu_buffer_rsrc_t rsrc_t;

constexpr int XA_LD = 4608;
constexpr size_t WS_XA = 0;
constexpr size_t WS_PQ = WS_XA + (size_t)2048 * XA_LD * 2;
constexpr size_t WS_KV = WS_PQ + (size_t)2048 * 1024 * 2;
constexpr size_t WS_Z  = WS_KV + (size_t)8 * 32 * 1024 * 4;
constexpr size_t WS_G  = WS_Z + (size_t)8 * 32 * 32 * 4;
constexpr size_t WS_EP    = WS_G + 2048 * 4;
constexpr size_t WS_DONE1 = WS_EP + 256;
constexpr size_t WS_ODONE = WS_DONE1 + 2048;
constexpr size_t WS_PDONE = WS_ODONE + 2048;
constexpr size_t WS_START = WS_PDONE + 128 * 128;
constexpr size_t WS_PART  = WS_START + 4096;

#define SCHED() __builtin_amdgcn_sched_barrier(0)
#define WAIT_V(n) asm volatile("s_waitcnt vmcnt(%0)" ::"n"(n) : "memory")
#define WAIT_L(n) asm volatile("s_waitcnt lgkmcnt(%0)" ::"n"(n) : "memory")
#define BAR() __builtin_amdgcn_s_barrier()

template <int OFF> DI void rd128(bf16x8& d, unsigned a) { asm volatile("ds_read_b128 %0, %1 offset:%2" : "=v"(d) : "v"(a), "i"(OFF) : "memory"); }
template <int OFF> DI void rdtr(s16x4& d, unsigned a) { asm volatile("ds_read_b64_tr_b16 %0, %1 offset:%2" : "=v"(d) : "v"(a), "i"(OFF) : "memory"); }
typedef __bf16 bf16x2_t __attribute__((ext_vector_type(2)));
DI void gload4(u32x4& d, const void* p) { asm volatile("global_load_dwordx4 %0, %1, off" : "=v"(d) : "v"(p) : "memory"); }
DI void gload1(float& d, const void* p) { asm volatile("global_load_dword %0, %1, off" : "=v"(d) : "v"(p) : "memory"); }
DI void gload2(u32x2& d, const void* p) { asm volatile("global_load_dwordx2 %0, %1, off" : "=v"(d) : "v"(p) : "memory"); }
template <class T> DI void pin(T& v) { asm volatile("" : "+v"(v)); }
template <class T> DI void keep(const T& v) { asm volatile("" :: "v"(v)); }
DI rsrc_t mk_rsrc(const void* p, unsigned n = 0x7fffffff) { return __builtin_amdgcn_make_buffer_rsrc((void*)p, 0, n, 0x00020000); }
DI void dma16(rsrc_t r, unsigned voff, unsigned soff, LAS char* l) { __builtin_amdgcn_raw_ptr_buffer_load_lds(r, (LAS void*)l, 16, voff, soff, 0, 0); }
DI unsigned cvt_pk(float lo, float hi) { unsigned r; asm("v_cvt_pk_bf16_f32 %0, %1, %2" : "=v"(r) : "v"(lo), "v"(hi)); return r; }
DI float bflo(unsigned w) { return __uint_as_float(w << 16); }
DI float bfhi(unsigned w) { return __uint_as_float(w & 0xffff0000u); }
constexpr float LOG2E = 1.44269504088896f;
DI float phi_f(float t) { const float e = __builtin_amdgcn_exp2f(__builtin_fminf(t * LOG2E, 0.f)); return e + __builtin_fmaxf(t + 1e-6f, 1e-6f); }
DI f32x2 mish2(f32x2 h) {
  const f32x2 t = h * LOG2E;
  f32x2 e; e.x = __builtin_amdgcn_exp2f(t.x); e.y = __builtin_amdgcn_exp2f(t.y);
  const f32x2 a = __builtin_elementwise_fma(e, (f32x2){0.5f, 0.5f}, (f32x2){1.f, 1.f});
  const f32x2 d = __builtin_elementwise_fma(e, a, (f32x2){1.f, 1.f});
  f32x2 r; r.x = __builtin_amdgcn_rcpf(d.x); r.y = __builtin_amdgcn_rcpf(d.y);
  return __builtin_elementwise_fma(-h, r, h);
}
DI f32x4 mish4(const f32x4& v) { const f32x2 a = mish2((f32x2){v[0], v[1]}), b = mish2((f32x2){v[2], v[3]}); return (f32x4){a.x, a.y, b.x, b.y}; }
DI f32x4 phi4(const f32x4& v) { return (f32x4){phi_f(v[0]), phi_f(v[1]), phi_f(v[2]), phi_f(v[3])}; }
#define MFMA16(a, b, c) __builtin_amdgcn_mfma_f32_16x16x32_bf16((a), (b), (c), 0, 0, 0)
#define CAT(L, H) __builtin_shufflevector((L), (H), 0, 1, 2, 3, 4, 5, 6, 7)
template <class F, int... I> DI void static_for_impl(F&& f, std::integer_sequence<int, I...>) { (f(std::integral_constant<int, I>{}), ...); }
template <int N, class F> DI void static_for(F&& f) { static_for_impl(f, std::make_integer_sequence<int, N>{}); }

constexpr int K1_XS = 32768, K1_WS = 26624, K1_WOFF = 3 * K1_XS;
constexpr int K2_XS = 16384, K2_WOFF = 98304, K2_SCR = 65536, LDS_BYTES = 163840;
#define K2_NA 26

struct K1Args {
  const u16* x; const u16* wqkv; const u16* w1; const u16* b1; const u16* gw; char* ws; const u16* w2; const u16* wo; u16* out;
};
typedef unsigned long long u64;
typedef __attribute__((address_space(1))) u64 gu64;
#define RLX_AGENT __ATOMIC_RELAXED, __HIP_MEMORY_SCOPE_AGENT
constexpr int SC1 = 16;
DI void touch(unsigned& dummy, const void* p) { asm volatile("global_load_dword %0, %1, off" : "+v"(dummy) : "v"(p) : "memory"); }
DI void poll_issue(u64& d, const void* p) { asm volatile("global_load_dwordx2 %0, %1, off sc1" : "=v"(d) : "v"(p) : "memory"); }
DI void wait_all(const void* p, u64 tag) {
  for (unsigned spins = 0; spins < (1u << 20); ++spins) {
    const u64 v = __hip_atomic_load((const gu64*)p, RLX_AGENT);
    if (__all(v == tag)) return;
    __builtin_amdgcn_s_sleep(1);
  }
}

template <bool KV, int WN> constexpr int blk_panel(int b) { return b == 6 ? 3 : (KV ? (b >> 1) : (b < 4 ? WN : 2)); }
template <bool KV, int WN> constexpr int blk_cb(int b) { return b == 6 ? 0 : (KV ? 2 * WN + (b & 1) : (b < 4 ? b : 2 * WN + (b & 1))); }
template <bool KV, int WN> constexpr bool blk_std(int b) { return KV && b < 4; }
template <bool KV, int WN> constexpr bool blk_bias(int b) { return KV ? b >= 4 : (WN == 1 || b >= 4); }

template <bool KV, int WN> DI void k1_epilogue(const K1Args& A, LAS char* lds, f32x4 (&acc)[4][7], int panel, int hp, int wm);
template <bool KV, int WN>
DI void k1_body(const K1Args& A, LAS char* lds, int panel, int hp, int j32, int wm, int lane) {
  constexpr int NB = 7;
  const int wid = WN * 4 + wm;
#define STAMP_AT(i)
  const int G = hp >> 1, a = hp & 1;
  const int g4 = lane >> 4, l15 = lane & 15;
  const int c0 = (KV ? 1024 : 0) + 64 * hp;
  const int c1 = KV ? (2048 + 64 * hp) : (448 * G + 128 + 128 * a);
  const int c2 = KV ? (448 * G + 64 * a) : (448 * G + 128 + 128 * a + 64);
  const int c3 = 448 * G + 384 + (KV ? 0 : 32) + 16 * a;
  auto pcol = [&](int P) { return P == 0 ? c0 : P == 1 ? c1 : P == 2 ? c2 : c3; };
  f32x4 acc[4][NB];
  static_for<NB>([&](auto B) {
    constexpr int b = B;
    f32x4 init = {0.f, 0.f, 0.f, 0.f};
    if constexpr (blk_bias<KV, WN>(b)) {
      const u32x2 w = *(const u32x2*)((const char*)A.b1 + (pcol(blk_panel<KV, WN>(b)) + 16 * blk_cb<KV, WN>(b) + 4 * g4) * 2);
      init = (f32x4){bflo(w.x), bfhi(w.x), bflo(w.y), bfhi(w.y)};
    }
#pragma unroll
    for (int mb = 0; mb < 4; ++mb) acc[mb][b] = init;
  });
  const unsigned ldsb = (unsigned)(size_t)lds;
  unsigned xcur = ldsb + (64 * wm + l15) * 128 + ((g4 ^ ((lane >> 1) & 7)) * 16);
  const int q4 = l15 >> 2, p4 = lane & 3;
  const int sr = ((q4 >> 1) & 1) | ((g4 & 1) << 1);
  unsigned aw[4];
#pragma unroll
  for (int c = 0; c < 4; ++c) aw[c] = ldsb + K1_WOFF + (8 * g4 + q4) * 128 + ((c ^ sr) * 32) + p4 * 8;
  unsigned an[2];
#pragma unroll
  for (int t = 0; t < 2; ++t) an[t] = ldsb + K1_WOFF + 24576 + (8 * g4 + ((4 * t) ^ (4 * (g4 & 1))) + q4) * 32 + p4 * 8;

  bf16x8 Af[4]; s16x4 Bl[NB], Bh[NB];
  auto readA = [&](auto MB, unsigned addr) { rd128<MB * 2048>(Af[MB], addr); };
  auto readBh = [&](auto WP, auto KS, auto B, auto H) {
    constexpr int b = B, P = blk_panel<KV, WN>(b), c = blk_cb<KV, WN>(b), wo = WP * K1_WS;
    if constexpr (P < 3) { if constexpr (H == 0) rdtr<wo + P * 8192 + KS * 4096>(Bl[b], aw[c]); else rdtr<wo + P * 8192 + KS * 4096 + 512>(Bh[b], aw[c]); }
    else { if constexpr (H == 0) rdtr<wo + KS * 1024>(Bl[b], an[0]); else rdtr<wo + KS * 1024>(Bh[b], an[1]); }
  };
  auto mma = [&](auto B, auto MB) {
    constexpr int b = B, mb = MB;
    if constexpr (blk_std<KV, WN>(b)) acc[mb][b] = MFMA16(Af[mb], CAT(Bl[b], Bh[b]), acc[mb][b]);
    else acc[mb][b] = MFMA16(CAT(Bl[b], Bh[b]), Af[mb], acc[mb][b]);
  };
  using I0 = std::integral_constant<int, 0>; using I1 = std::integral_constant<int, 1>;
  using I2 = std::integral_constant<int, 2>; using I3 = std::integral_constant<int, 3>;
  using I5 = std::integral_constant<int, 5>; using I6 = std::integral_constant<int, 6>;
  auto h1_reads = [&](auto U, auto WP, auto KS, unsigned xa_) {
    constexpr int u = U;
    if constexpr (WN == 0) {
      if constexpr (u == 0) { readA(I0{}, xa_); readBh(WP, KS, I6{}, I0{}); }
      else if constexpr (u == 1) { readA(I1{}, xa_); readBh(WP, KS, I6{}, I1{}); }
      else readBh(WP, KS, std::integral_constant<int, ((u - 2) >> 1)>{}, std::integral_constant<int, (u & 1)>{});
    } else {
      if constexpr (u == 0) readA(I0{}, xa_);
      else if constexpr (u == 1) readA(I1{}, xa_);
      else if constexpr (u < 4) readBh(WP, KS, I6{}, std::integral_constant<int, (u & 1)>{});
      else readBh(WP, KS, std::integral_constant<int, ((u - 4) >> 1)>{}, std::integral_constant<int, (u & 1)>{});
    }
  };
  constexpr int L0 = WN == 0 ? 14 : 12, L1 = 26 - L0;
  auto step = [&](auto WP, auto KS, unsigned xcur, auto WPN, auto KSN, unsigned xnext, auto BARRIER) {
    static_for<L0>([&](auto S) {
      constexpr int s = S;
      if constexpr (s == 0 || s == 2 || s == 4) WAIT_L(8);
      if constexpr (s == 6) WAIT_L(6);
      if constexpr (s == 8) WAIT_L(4);
      if constexpr (s == 10) WAIT_L(2);
      if constexpr ((s & 1) == 0) SCHED();
      if constexpr (s < 12) mma(std::integral_constant<int, (s >> 1)>{}, std::integral_constant<int, (s & 1)>{}); else mma(I6{}, std::integral_constant<int, (s & 1)>{});
      if constexpr (s == 0) readBh(WP, KS, I5{}, I0{});
      if constexpr (s == 1) readBh(WP, KS, I5{}, I1{});
      if constexpr (s == 2) readA(I2{}, xcur);
      if constexpr (s == 3) readA(I3{}, xcur);
      SCHED();
    });
    WAIT_L(0);
    if constexpr (decltype(BARRIER)::value) {
      BAR();
      if constexpr (WN == 0) __builtin_amdgcn_s_setprio(2);
    }
    else { if constexpr (WN == 0) __builtin_amdgcn_s_setprio(0); }
    SCHED();
    static_for<L1>([&](auto U) {
      constexpr int u = U;
      if constexpr (WN == 0) mma(std::integral_constant<int, (u >> 1)>{}, std::integral_constant<int, 2 + (u & 1)>{});
      else { if constexpr (u < 2) mma(I6{}, std::integral_constant<int, 2 + u>{}); else mma(std::integral_constant<int, ((u - 2) >> 1)>{}, std::integral_constant<int, 2 + (u & 1)>{}); }
      h1_reads(U, WPN, KSN, xnext);
      SCHED();
    });
  };
  STAMP_AT(1);
  BAR(); SCHED();
  STAMP_AT(2);
  static_for<L1>([&](auto U) { h1_reads(U, I0{}, I0{}, xcur); });
  if constexpr (WN == 0) __builtin_amdgcn_s_setprio(2); else __builtin_amdgcn_s_setprio(1);
  int xs = 0;
  auto ktile = [&](auto WP, int t) {
    unsigned x1 = xcur ^ 64u, xn = xcur + (xs == 2 ? (unsigned)(-2 * K1_XS) : (unsigned)K1_XS);
    pin(x1); pin(xn);
    step(WP, I0{}, xcur, WP, I1{}, x1, std::false_type{});
    step(WP, I1{}, x1, std::integral_constant<int, WP ^ 1>{}, I0{}, xn, std::true_type{});
    xcur = xn; xs = xs == 2 ? 0 : xs + 1;
  };
#pragma unroll 1
  for (int t = 0; t < 16; t += 2) { ktile(I0{}, t); ktile(I1{}, t + 1); }
  SCHED(); WAIT_L(0); SCHED();
  __builtin_amdgcn_s_setprio(0);
  STAMP_AT(3);

  k1_epilogue<KV, WN>(A, lds, acc, panel, hp, wm);
  WAIT_V(0);
}

template <bool KV, int WN>
DI void k1_epilogue(const K1Args& A, LAS char* lds, f32x4 (&acc)[4][7], int panel, int hp, int wm) {
  int lane = threadIdx.x & 63; asm volatile("" : "+v"(lane));
  const int g4 = lane >> 4, l15 = lane & 15;
  const int G = hp >> 1, a = hp & 1;
  const int c0 = (KV ? 1024 : 0) + 64 * hp;
  const int c1 = KV ? (2048 + 64 * hp) : (448 * G + 128 + 128 * a);
  const int c2 = KV ? (448 * G + 64 * a) : (448 * G + 128 + 128 * a + 64);
  const int c3 = 448 * G + 384 + (KV ? 0 : 32) + 16 * a;
  using I0 = std::integral_constant<int, 0>; using I1 = std::integral_constant<int, 1>;
  u16* xa = (u16*)(A.ws + WS_XA);
  const size_t rowbase = (size_t)panel * 256 + 64 * wm;
  LAS char* stg = lds + (WN * 4 + wm) * 8192;
  const int chunk_g = (g4 & 1) * 2 + (g4 >> 1);
  auto store_line = [&](auto B0, auto ACT, u16* base, size_t ld, int col0) {
    static_for<4>([&](auto MB) {
      constexpr int mb = MB;
      static_for<2>([&](auto PP) {
        constexpr int p = PP;
        f32x4 e = acc[mb][B0 + 2 * p], o = acc[mb][B0 + 2 * p + 1];
        if constexpr (ACT == 0) { e = phi4(e); o = phi4(o); } else { e = mish4(e); o = mish4(o); }
        const unsigned e0 = cvt_pk(e[0], e[1]), e1 = cvt_pk(e[2], e[3]), o0 = cvt_pk(o[0], o[1]), o1 = cvt_pk(o[2], o[3]);
        const auto r0 = __builtin_amdgcn_permlane16_swap(e0, o0, false, false);
        const auto r1 = __builtin_amdgcn_permlane16_swap(e1, o1, false, false);
        const u32x4 w = {r0[0], r1[0], r0[1], r1[1]};
        *(LAS u32x4*)(stg + (16 * mb + l15) * 128 + (((4 * p + chunk_g) ^ (l15 & 7)) * 16)) = w;
      });
    });
#pragma unroll
    for (int i = 0; i < 8; ++i) {
      const u32x4 w = *(LAS u32x4*)(stg + i * 1024 + lane * 16);
      const int r = 8 * i + (lane >> 3), ch = (lane & 7) ^ (r & 7);
      __builtin_amdgcn_raw_buffer_store_b128(w, mk_rsrc(base), (unsigned)(((rowbase + r) * ld + col0 + ch * 8) * 2), 0, SC1);
    }
  };
  auto store_half = [&](int col0) {
#pragma unroll
    for (int mb = 0; mb < 4; ++mb) {
      const f32x4 e = mish4(acc[mb][4]), o = mish4(acc[mb][5]);
      const unsigned e0 = cvt_pk(e[0], e[1]), e1 = cvt_pk(e[2], e[3]), o0 = cvt_pk(o[0], o[1]), o1 = cvt_pk(o[2], o[3]);
      const auto r0 = __builtin_amdgcn_permlane16_swap(e0, o0, false, false);
      const auto r1 = __builtin_amdgcn_permlane16_swap(e1, o1, false, false);
      const u32x4 w = {r0[0], r1[0], r0[1], r1[1]};
      __builtin_amdgcn_raw_buffer_store_b128(w, mk_rsrc(xa), (unsigned)(((rowbase + 16 * mb + l15) * XA_LD + col0 + chunk_g * 8) * 2), 0, SC1);
    }
  };
  auto store_narrow = [&]() {
#pragma unroll
    for (int m2 = 0; m2 < 2; ++m2) {
      const int mb = 2 * WN + m2;
      const f32x4 v = mish4(acc[2 * WN + m2][6]);
      const u32x2 w = {cvt_pk(v[0], v[1]), cvt_pk(v[2], v[3])};
      __builtin_amdgcn_raw_buffer_store_b64(w, mk_rsrc(xa), (unsigned)(((rowbase + 16 * mb + l15) * XA_LD + c3 + 4 * g4) * 2), 0, SC1);
    }
  };
  store_half(c2 + 32 * WN);
  store_narrow();
  if constexpr (!KV) {
    if constexpr (WN == 0) store_line(I0{}, I0{}, (u16*)(A.ws + WS_PQ), 1024, c0);
    else store_line(I0{}, I1{}, xa, XA_LD, c1);
  } else {
    f32x4 kvD[4]; f32x4 zD[2];
#pragma unroll
    for (int i = 0; i < 4; ++i) kvD[i] = (f32x4){0.f, 0.f, 0.f, 0.f};
    zD[0] = zD[1] = (f32x4){0.f, 0.f, 0.f, 0.f};
    const bf16x8 ones = {0x3f80, 0x3f80, 0x3f80, 0x3f80, 0x3f80, 0x3f80, 0x3f80, 0x3f80};
#pragma unroll
    for (int mp = 0; mp < 2; ++mp) {
      bf16x8 ka[2], vb[2];
#pragma unroll
      for (int b = 0; b < 2; ++b) {
        const f32x4 k0 = phi4(acc[2 * mp][b]), k1 = phi4(acc[2 * mp + 1][b]);
        const u32x4 w = {cvt_pk(k0[0], k0[1]), cvt_pk(k0[2], k0[3]), cvt_pk(k1[0], k1[1]), cvt_pk(k1[2], k1[3])};
        ka[b] = __builtin_bit_cast(bf16x8, w);
        const f32x4 v0 = acc[2 * mp][2 + b], v1 = acc[2 * mp + 1][2 + b];
        const u32x4 u = {cvt_pk(v0[0], v0[1]), cvt_pk(v0[2], v0[3]), cvt_pk(v1[0], v1[1]), cvt_pk(v1[2], v1[3])};
        vb[b] = __builtin_bit_cast(bf16x8, u);
      }
#pragma unroll
      for (int db = 0; db < 2; ++db) {
#pragma unroll
        for (int eb = 0; eb < 2; ++eb) kvD[db * 2 + eb] = MFMA16(ka[db], vb[eb], kvD[db * 2 + eb]);
        zD[db] = MFMA16(ka[db], ones, zD[db]);
      }
    }
    LAS char* sc = lds + 2 * K1_XS;
    LAS char* sz = lds;
#pragma unroll
    for (int i = 0; i < 4; ++i) *(LAS f32x4*)(sc + ((WN * 4 + wm) * 4 + i) * 1024 + lane * 16) = kvD[i];
    if (l15 == 0) {
#pragma unroll
      for (int db = 0; db < 2; ++db) *(LAS f32x4*)(sz + ((WN * 4 + wm) * 2 + db) * 64 + g4 * 16) = zD[db];
    }
    WAIT_L(0); BAR(); SCHED();
    const int blk = wm;
    f32x4 s = {0.f, 0.f, 0.f, 0.f};
#pragma unroll
    for (int m = 0; m < 4; ++m) s += *(LAS f32x4*)(sc + ((WN * 4 + m) * 4 + blk) * 1024 + lane * 16);
    __builtin_amdgcn_raw_buffer_store_b128(__builtin_bit_cast(u32x4, s), mk_rsrc(A.ws + WS_KV),
        (unsigned)(((panel * 32 + 2 * hp + WN) * 1024 + (16 * (blk & 1) + l15) * 32 + 16 * (blk >> 1) + 4 * g4) * 4), 0, SC1);
    if (wm == 0 && lane < 8) {
      const int db = lane >> 2, gq = lane & 3;
      f32x4 zs = {0.f, 0.f, 0.f, 0.f};
#pragma unroll
      for (int m = 0; m < 4; ++m) zs += *(LAS f32x4*)(sz + ((WN * 4 + m) * 2 + db) * 64 + gq * 16);
      __builtin_amdgcn_raw_buffer_store_b128(__builtin_bit_cast(u32x4, zs), mk_rsrc(A.ws + WS_Z), (unsigned)(((panel * 32 + 2 * hp + WN) * 32 + 16 * db + 4 * gq) * 4), 0, SC1);
    }
  }
}

DI void k2_compute(const K1Args& A, LAS char* lds, int tm, int tn, int role, int panel, int hh, int wid) {
  int lane = threadIdx.x & 63; asm volatile("" : "+v"(lane));
  const int nt = role == 0 ? K2_NA + 16 : 56 - K2_NA, tile = tm * 8 + tn;
  const int wm = wid >> 2, wn = wid & 3;
  const int g4 = lane >> 4, l15 = lane & 15;
  const unsigned prow = (unsigned)panel * 256 + 32 * wid;
  const rsrc_t rws = mk_rsrc(A.ws);
  u32x2 part[8]; u32x4 pf[2], zq; unsigned gv[2], gq[4];
#pragma unroll
  for (int q = 0; q < 8; ++q) part[q] = __builtin_amdgcn_raw_buffer_load_b64(rws, (unsigned)(WS_KV + ((q * 32 + hh) * 1024 + (wid * 64 + lane) * 2) * 4), 0, SC1);
  zq = __builtin_amdgcn_raw_buffer_load_b128(rws, (unsigned)(WS_Z + ((lane >> 3) * 1024 + hh * 32 + (lane & 7) * 4) * 4), 0, SC1);
#pragma unroll
  for (int mb = 0; mb < 2; ++mb) {
    pf[mb] = __builtin_amdgcn_raw_buffer_load_b128(rws, (unsigned)(WS_PQ + ((prow + 16 * mb + l15) * 1024 + 32 * hh + 8 * g4) * 2), 0, SC1);
    gv[mb] = __builtin_amdgcn_raw_buffer_load_b32(rws, (unsigned)(WS_G + (prow + 16 * mb + l15) * 4), 0, SC1);
  }
#pragma unroll
  for (int mb = 0; mb < 4; ++mb) gq[mb] = __builtin_amdgcn_raw_buffer_load_b32(rws, (unsigned)(WS_G + (tm * 128 + 64 * wm + 16 * mb + l15) * 4), 0, SC1);
  const unsigned ldsb = (unsigned)(size_t)lds;
  unsigned ax[2];
#pragma unroll
  for (int ks = 0; ks < 2; ++ks) ax[ks] = ldsb + (64 * wm + l15) * 128 + (((4 * ks + g4) ^ ((lane >> 1) & 7)) * 16);
  const int q4 = l15 >> 2, p4 = lane & 3;
  const int sr = ((q4 >> 1) & 1) | ((g4 & 1) << 1);
  unsigned aw[2];
#pragma unroll
  for (int nb = 0; nb < 2; ++nb) aw[nb] = ldsb + K2_WOFF + (wn >> 1) * 8192 + (8 * g4 + q4) * 128 + (((2 * (wn & 1) + nb) ^ sr) * 32) + p4 * 8;
  f32x4 acc[4][2];
#pragma unroll
  for (int i = 0; i < 4; ++i) acc[i][0] = acc[i][1] = (f32x4){0.f, 0.f, 0.f, 0.f};
  bf16x8 Af[2][2][4]; s16x4 Bl[2][2][2], Bh[2][2][2];
  auto rd = [&](auto BUF, auto I, const unsigned (&pa)[2], const unsigned (&pw)[2]) {
    constexpr int i = I, ks = i >> 3, r = i & 7;
    if constexpr (r < 2) { if constexpr (r == 0) rdtr<ks * 4096>(Bl[BUF][ks][0], pw[0]); else rdtr<ks * 4096 + 512>(Bh[BUF][ks][0], pw[0]); }
    else if constexpr (r < 6) rd128<(r - 2) * 2048>(Af[BUF][ks][r - 2], pa[ks]);
    else { if constexpr (r == 6) rdtr<ks * 4096>(Bl[BUF][ks][1], pw[1]); else rdtr<ks * 4096 + 512>(Bh[BUF][ks][1], pw[1]); }
  };
  using I0 = std::integral_constant<int, 0>; using I1 = std::integral_constant<int, 1>;
  LAS char* kvs = lds + K2_SCR;
  {
    float s0 = 0.f, s1 = 0.f;
#pragma unroll
    for (int q = 0; q < 8; ++q) { s0 += __uint_as_float(part[q].x); s1 += __uint_as_float(part[q].y); }
    *(LAS unsigned*)(kvs + (wid * 64 + lane) * 4) = cvt_pk(s0, s1);
    if (wid == 0) {
      f32x4 z = __builtin_bit_cast(f32x4, zq);
#pragma unroll
      for (int o = 8; o <= 32; o <<= 1) { z[0] += __shfl_xor(z[0], o); z[1] += __shfl_xor(z[1], o); z[2] += __shfl_xor(z[2], o); z[3] += __shfl_xor(z[3], o); }
      if (lane < 8) *(LAS u32x2*)(kvs + 2048 + lane * 8) = (u32x2){cvt_pk(z[0], z[1]), cvt_pk(z[2], z[3])};
    }
    SCHED();
  }
  STAMP_AT(1);
  BAR(); SCHED();
  STAMP_AT(2);
  static_for<16>([&](auto I) { rd(I0{}, I, ax, aw); });
  auto body = [&](auto CUR, int i) {
    constexpr int cur = CUR;
    const unsigned so1 = ((i + 1) & 3) * K2_XS;
    unsigned pa[2] = {ax[0] + so1, ax[1] + so1}, pw[2] = {aw[0] + so1, aw[1] + so1};
    pin(pa[0]); pin(pa[1]); pin(pw[0]); pin(pw[1]);
    WAIT_L(0); BAR(); SCHED();
    static_for<16>([&](auto S) {
      constexpr int s = S, ks = s >> 3, nb = (s >> 2) & 1, mb = s & 3;
      acc[mb][nb] = MFMA16(CAT(Bl[cur][ks][nb], Bh[cur][ks][nb]), Af[cur][ks][mb], acc[mb][nb]);
      if constexpr (s < 8) { rd(std::integral_constant<int, cur ^ 1>{}, S, pa, pw); }
      else if constexpr (s < 12) { rd(std::integral_constant<int, cur ^ 1>{}, std::integral_constant<int, 2 * s - 8>{}, pa, pw); rd(std::integral_constant<int, cur ^ 1>{}, std::integral_constant<int, 2 * s - 7>{}, pa, pw); }
      SCHED();
    });
  };
  body(I0{}, 0);
  {
    bf16x8 ka[2], za;
    const unsigned kb = (unsigned)(size_t)kvs;
    rd128<0>(ka[0], kb + l15 * 64 + g4 * 16); rd128<1024>(ka[1], kb + l15 * 64 + g4 * 16); rd128<2048>(za, kb + g4 * 16);
    WAIT_L(0); SCHED();
    const int chunk_g = (g4 & 1) * 2 + (g4 >> 1);
#pragma unroll
    for (int mb = 0; mb < 2; ++mb) {
      const f32x4 zero = {0.f, 0.f, 0.f, 0.f};
      const bf16x8 pb = __builtin_bit_cast(bf16x8, pf[mb]);
      const f32x4 n0 = MFMA16(ka[0], pb, zero), n1 = MFMA16(ka[1], pb, zero), dn = MFMA16(za, pb, zero);
      const float sc = __uint_as_float(gv[mb]) * __builtin_amdgcn_rcpf(dn[0] + 1e-6f);
      const f32x4 e = n0 * sc, o = n1 * sc;
      const unsigned e0 = cvt_pk(e[0], e[1]), e1 = cvt_pk(e[2], e[3]), o0 = cvt_pk(o[0], o[1]), o1 = cvt_pk(o[2], o[3]);
      const auto r0 = __builtin_amdgcn_permlane16_swap(e0, o0, false, false);
      const auto r1 = __builtin_amdgcn_permlane16_swap(e1, o1, false, false);
      const u32x4 w = {r0[0], r1[0], r0[1], r1[1]};
      __builtin_amdgcn_raw_buffer_store_b128(w, rws, (unsigned)(WS_XA + ((prow + 16 * mb + l15) * XA_LD + 3584 + 32 * hh + chunk_g * 8) * 2), 0, SC1);
    }
    SCHED();
  }
  body(I1{}, 1);
  body(I0{}, 2);
  float omg[4];
#pragma unroll
  for (int mb = 0; mb < 4; ++mb) omg[mb] = 1.f - __uint_as_float(gq[mb]);
  body(I1{}, 3);
  WAIT_V(0);
  WAIT_L(0); SCHED();
  const int n1 = role == 0 ? K2_NA : nt;
#pragma unroll 1
  for (int i = 4; i < n1; i += 2) { body(I0{}, i); body(I1{}, i + 1); }
  SCHED(); WAIT_L(0); SCHED();
#pragma unroll
  for (int mb = 0; mb < 4; ++mb) { acc[mb][0] *= omg[mb]; acc[mb][1] *= omg[mb]; }
#pragma unroll 1
  for (int i = n1; i < nt; i += 2) { body(I0{}, i); body(I1{}, i + 1); }
  SCHED(); WAIT_L(0); SCHED();
  STAMP_AT(3);
  if (role == 1) {
    const unsigned poff = (unsigned)(WS_PART + (tile * 8 + wid) * 8192 + lane * 16);
#pragma unroll
    for (int mb = 0; mb < 4; ++mb)
#pragma unroll
      for (int nb = 0; nb < 2; ++nb) __builtin_amdgcn_raw_buffer_store_b128(__builtin_bit_cast(u32x4, acc[mb][nb]), rws, poff + (mb * 2 + nb) * 1024, 0, SC1);
    STAMP_AT(4);
    WAIT_V(0);
    BAR();
  } else {
    BAR();
    BAR();
    STAMP_AT(4);
    const int chunk_g = (g4 & 1) * 2 + (g4 >> 1);
    LAS char* pl = lds + (wid < 4 ? K2_SCR + wid * 8192 : (nt & 3) * K2_XS + (wid - 4) * 8192) + lane * 16;
#pragma unroll
    for (int mb = 0; mb < 4; ++mb) {
      const f32x4 e = acc[mb][0] + *(LAS f32x4*)(pl + (mb * 2) * 1024), o = acc[mb][1] + *(LAS f32x4*)(pl + (mb * 2 + 1) * 1024);
      const unsigned e0 = cvt_pk(e[0], e[1]), e1 = cvt_pk(e[2], e[3]), o0 = cvt_pk(o[0], o[1]), o1 = cvt_pk(o[2], o[3]);
      const auto r0 = __builtin_amdgcn_permlane16_swap(e0, o0, false, false);
      const auto r1 = __builtin_amdgcn_permlane16_swap(e1, o1, false, false);
      const u32x4 w = {r0[0], r1[0], r0[1], r1[1]};
      *(u32x4*)(A.out + (size_t)(tm * 128 + 64 * wm + 16 * mb + l15) * 1024 + tn * 128 + 32 * wn + chunk_g * 8) = w;
    }
  }
}

DI void loader(const K1Args& A, LAS char* lds, bool isx, int lane) {
  const int b = blockIdx.x, xcd = b & 7, idx = b >> 3, rq = xcd >> 1, role = xcd & 1;
  const int panel = 2 * rq + (idx >> 4), c16 = idx & 15, hp = 8 * role + (c16 >> 1), j32 = role * 16 + c16;
  const bool kv = (c16 & 1) != 0;
  u64 ep; poll_issue(ep, A.ws + WS_EP);
  const int G = hp >> 1, a = hp & 1;
  const int c0 = (kv ? 1024 : 0) + 64 * hp;
  const int c1 = kv ? (2048 + 64 * hp) : (448 * G + 128 + 128 * a);
  const int c2 = kv ? (448 * G + 64 * a) : (448 * G + 128 + 128 * a + 64);
  const int c3 = 448 * G + 384 + (kv ? 0 : 32) + 16 * a;
  unsigned chx[2], chw[2];
#pragma unroll
  for (int par = 0; par < 2; ++par) {
    chx[par] = (unsigned)(((lane & 7) ^ ((4 * par + (lane >> 4)) & 7)) * 16);
    chw[par] = (unsigned)(((lane & 7) ^ ((((lane >> 4) & 1) | (par << 1)) << 1)) * 16);
  }
  unsigned tdummy = 0;
  u64 tag;
  {
    unsigned vA[4], vB[4], vN[2];
#pragma unroll
    for (int k = 0; k < 4; ++k) {
      vA[k] = (isx ? (unsigned)((8 * k + (lane >> 3)) * 2048) + chx[k & 1] : (unsigned)((8 * k + (lane >> 3)) * 6144) + chw[k & 1]) + 3072 - k * 1024;
      vB[k] = (unsigned)((8 * k + (lane >> 3)) * 7168) + chw[k & 1] + 3072 - k * 1024;
    }
#pragma unroll
    for (int n = 0; n < 2; ++n) { const int rho = 32 * n + (lane >> 1); vN[n] = (unsigned)((rho ^ (((rho >> 3) & 1) << 2)) * 7168 + (lane & 1) * 16) + 3072 - n * 1024; }
    const rsrc_t rx = mk_rsrc((const char*)A.x - 3072), rqk = mk_rsrc((const char*)A.wqkv - 3072), r1 = mk_rsrc((const char*)A.w1 - 3072);
    const unsigned xrow0 = (unsigned)panel * 256 * 2048;
    auto issueX = [&](int t, int xs) {
#pragma unroll
      for (int g = 0; g < 8; ++g)
        static_for<4>([&](auto K) { __builtin_amdgcn_raw_ptr_buffer_load_lds(rx, (LAS void*)(lds + xs * K1_XS + g * 4096), 16, vA[K], xrow0 + t * 128 + g * 65536, K * 1024, 0); });
    };
    auto issueW = [&](int t) {
      LAS char* dst = lds + K1_WOFF + (t & 1) * K1_WS;
#pragma unroll
      for (int g = 0; g < 2; ++g)
        static_for<4>([&](auto K) { __builtin_amdgcn_raw_ptr_buffer_load_lds(rqk, (LAS void*)(dst + g * 4096), 16, vA[K], (t * 64 + g * 32) * 6144 + c0 * 2, K * 1024, 0); });
      if (kv) {
#pragma unroll
        for (int g = 0; g < 2; ++g)
          static_for<4>([&](auto K) { __builtin_amdgcn_raw_ptr_buffer_load_lds(rqk, (LAS void*)(dst + 8192 + g * 4096), 16, vA[K], (t * 64 + g * 32) * 6144 + c1 * 2, K * 1024, 0); });
      } else {
#pragma unroll
        for (int g = 0; g < 2; ++g)
          static_for<4>([&](auto K) { __builtin_amdgcn_raw_ptr_buffer_load_lds(r1, (LAS void*)(dst + 8192 + g * 4096), 16, vB[K], (t * 64 + g * 32) * 7168 + c1 * 2, K * 1024, 0); });
      }
#pragma unroll
      for (int g = 0; g < 2; ++g)
        static_for<4>([&](auto K) { __builtin_amdgcn_raw_ptr_buffer_load_lds(r1, (LAS void*)(dst + 16384 + g * 4096), 16, vB[K], (t * 64 + g * 32) * 7168 + c2 * 2, K * 1024, 0); });
      static_for<2>([&](auto N) { __builtin_amdgcn_raw_ptr_buffer_load_lds(r1, (LAS void*)(dst + 24576), 16, vN[N], t * 64 * 7168 + c3 * 2, N * 1024, 0); });
    };
    auto touch3 = [&]() {
      const int w = b * 2 + (isx ? 0 : 1);
#pragma unroll
      for (int n = 0; n < 3; ++n) {
        const int k = n * 64 + lane, li = w * 144 + (k < 144 ? k : 143);
        touch(tdummy, li < 57344 ? (const char*)A.w2 + (size_t)li * 128 : (const char*)A.wo + (size_t)(li - 57344) * 128);
      }
    };
    if (isx) {
      issueX(0, 0); issueX(1, 1);
#pragma unroll
      for (int n = 0; n < 4; ++n) touch(tdummy, (const char*)A.x + (size_t)(panel * 256 + 16 * c16 + 4 * n + (lane >> 4)) * 2048 + (lane & 15) * 128);
      WAIT_V(36); pin(ep); tag = ep + 1;
      BAR();
      if (lane == 0) __hip_atomic_store((gu64*)(A.ws + WS_START) + b, tag, RLX_AGENT);
      issueX(2, 2);
      int xs = 0;
      auto xiter = [&](int t) {
        WAIT_V(32); BAR(); issueX(t + 3, xs); xs = xs == 2 ? 0 : xs + 1;
        if (t < 7) {
          const int li = b * 52 + (lane < 52 ? lane : 51);
          int row = 64 * (2 * t + 3) + li / 104; row = row < 1024 ? row : 1023;
          const int c = li % 104;
          touch(tdummy, c < 48 ? (const char*)A.wqkv + (size_t)row * 6144 + c * 128 : (const char*)A.w1 + (size_t)row * 7168 + (c - 48) * 128);
        }
      };
#pragma unroll 1
      for (int t = 0; t < 8; ++t) xiter(t);
      u32x4 gx[8][2], gg[2];
#pragma unroll
      for (int r = 0; r < 8; ++r) { const char* xr = (const char*)A.x + (size_t)(panel * 256 + j32 * 8 + r) * 2048 + lane * 32; gload4(gx[r][0], xr); gload4(gx[r][1], xr + 16); }
      gload4(gg[0], (const char*)A.gw + lane * 32); gload4(gg[1], (const char*)A.gw + lane * 32 + 16);
      xiter(8); xiter(9);
      WAIT_V(32);
      {
        pin(gg[0]); pin(gg[1]);
        float s[8];
#pragma unroll
        for (int r = 0; r < 8; ++r) {
          pin(gx[r][0]); pin(gx[r][1]);
          s[r] = 0.f;
#pragma unroll
          for (int h = 0; h < 2; ++h)
#pragma unroll
            for (int i = 0; i < 4; ++i) { const unsigned ux = gx[r][h][i], ug = gg[h][i]; s[r] = __builtin_amdgcn_fdot2_f32_bf16(__builtin_bit_cast(bf16x2_t, ux), __builtin_bit_cast(bf16x2_t, ug), s[r], false); }
        }
#pragma unroll
        for (int o = 32; o >= 1; o >>= 1)
#pragma unroll
          for (int r = 0; r < 8; ++r) s[r] += __shfl_xor(s[r], o);
        float mine = s[0];
#pragma unroll
        for (int r = 1; r < 8; ++r) mine = lane == r ? s[r] : mine;
        const float gval = __builtin_amdgcn_rcpf(1.f + __builtin_amdgcn_exp2f(-LOG2E * mine));
        if (lane < 8) __hip_atomic_store((__attribute__((address_space(1))) unsigned*)(A.ws + WS_G) + panel * 256 + j32 * 8 + lane, __float_as_uint(gval), RLX_AGENT);
      }
#pragma unroll 1
      for (int t = 10; t < 13; ++t) xiter(t);
      touch3();
      WAIT_V(35); BAR();
      WAIT_V(3); BAR();
      BAR();
    } else {
      issueW(0); issueW(1);
      WAIT_V(26); pin(ep); tag = ep + 1;
      BAR();
#pragma unroll 1
      for (int t = 0; t < 14; ++t) { WAIT_V(0); BAR(); issueW(t + 2); }
      touch3();
      WAIT_V(3); BAR();
      BAR();
    }
  }
  const int tm = 4 * rq + (idx >> 3), tn = idx & 7, tile = tm * 8 + tn, hh = 16 * role + (idx & 15);
  const int nt = role == 0 ? K2_NA + 16 : 56 - K2_NA;
  unsigned voff[4];
#pragma unroll
  for (int k = 0; k < 4; ++k)
    voff[k] = (isx ? (unsigned)((8 * k + (lane >> 3)) * (XA_LD * 2) + chx[k & 1]) : (unsigned)((8 * k + (lane >> 3)) * 2048 + chw[k & 1])) + 3072 - k * 1024;
  const unsigned gstep = isx ? 32 * XA_LD * 2 : 32 * 2048;
  auto issue = [&](rsrc_t r, unsigned so, int slot) {
    LAS char* st = lds + (isx ? 0 : K2_WOFF) + slot * K2_XS;
#pragma unroll
    for (int g = 0; g < 4; ++g) {
      const unsigned sg = isx ? so + g * gstep : so + (g & 1) * gstep + (g >> 1) * 128;
      static_for<4>([&](auto K) { __builtin_amdgcn_raw_ptr_buffer_load_lds(r, (LAS void*)(st + g * 4096), 16, voff[K], sg, K * 1024, SC1); });
    }
  };
  const rsrc_t r0 = mk_rsrc(isx ? (const char*)(A.ws + WS_XA) - 3072 : (const char*)A.w2 - 3072);
  const rsrc_t r1 = mk_rsrc(isx ? (const char*)(A.ws + WS_XA) - 3072 : (const char*)A.wo - 3072);
  const unsigned tstep = isx ? 128u : 64u * 2048u;
  const unsigned sbase = isx ? (unsigned)tm * 128 * (XA_LD * 2) : (unsigned)tn * 256;
  const unsigned s0 = sbase + (role == 0 ? 0 : K2_NA) * tstep;
  const unsigned s1 = isx ? sbase + 56 * tstep : sbase;
  const rsrc_t rp = mk_rsrc(A.ws + WS_PART);
  auto issue_partial = [&]() {
    LAS char* dst = lds + (isx ? K2_SCR : (nt & 3) * K2_XS);
#pragma unroll
    for (int g = 0; g < 8; ++g)
      static_for<4>([&](auto K) { __builtin_amdgcn_raw_ptr_buffer_load_lds(rp, (LAS void*)(dst + g * 4096), 16, lane * 16, (unsigned)tile * 65536 + (isx ? 0 : 32768) + g * 4096, K * 1024, SC1); });
  };
  if (!isx) { issue(r0, s0, 0); issue(r0, s0 + tstep, 1); issue(r0, s0 + 2 * tstep, 2); issue(r0, s0 + 3 * tstep, 3); }
  if (kv) BAR();
  if (isx) WAIT_V(0);
  BAR();
  if (isx) {
    if (lane == 0) __hip_atomic_store((gu64*)(A.ws + WS_DONE1) + panel * 32 + j32, tag, RLX_AGENT);
    const int hk = hh >> 1, jkv = 16 * (hk >> 3) + 2 * (hk & 7) + 1;
    const int slot = lane < 32 ? panel * 32 + lane : (lane < 40 ? (lane - 32) * 32 + jkv : panel * 32);
    wait_all(A.ws + WS_DONE1 + (size_t)slot * 8, tag);
  }
  BAR();
  if (isx) { issue(r0, s0, 0); issue(r0, s0 + tstep, 1); issue(r0, s0 + 2 * tstep, 2); WAIT_V(32); } else WAIT_V(48);
  BAR();
  if (isx) issue(r0, s0 + 3 * tstep, 3);
  auto run = [&](int i0, int i1, rsrc_t r, unsigned so) {
#pragma unroll 1
    for (int i = i0; i < i1; ++i, so += tstep) { WAIT_V(32); BAR(); issue(r, so, i & 3); }
  };
  u64 pv = 0;
  run(0, 5, r0, s0 + 4 * tstep);
  if (isx && lane == 0) __hip_atomic_store((gu64*)(A.ws + WS_ODONE) + panel * 32 + hh, tag, RLX_AGENT);
  if (role == 0) {
    run(5, K2_NA - 8, r0, s0 + 9 * tstep);
    const char* od = A.ws + WS_ODONE + (size_t)(panel * 32 + (lane & 31)) * 8;
    if (isx) poll_issue(pv, (const void*)od);
    run(K2_NA - 8, K2_NA - 4, r0, s0 + (K2_NA - 4) * tstep);
    if (isx) { WAIT_V(32); pin(pv); if (!__all(pv == tag)) wait_all((const void*)od, tag); }
    run(K2_NA - 4, nt - 4, r1, s1);
  } else run(5, nt - 4, r0, s0 + 9 * tstep);
  const char* pd = A.ws + WS_PDONE + (size_t)tile * 128;
  poll_issue(pv, (const void*)pd);
  WAIT_V(33); BAR();
  WAIT_V(17); BAR();
  WAIT_V(0);
  bool early = false;
  if (role == 0) { pin(pv); early = __all(pv == tag); }
  BAR();
  if (early) issue_partial();
  BAR();
  if (role == 0) {
    if (!early) wait_all((const void*)pd, tag);
    BAR();
    if (!early) issue_partial();
    WAIT_V(0);
    BAR();
  } else {
    BAR();
    if (isx && lane == 0) __hip_atomic_store((gu64*)(A.ws + WS_PDONE + (size_t)tile * 128), tag, RLX_AGENT);
  }
  if (b == 0 && isx) {
#pragma unroll 1
    for (int k = 0; k < 4; ++k) wait_all(A.ws + WS_START + (size_t)(k * 64 + lane) * 8, tag);
    if (lane == 0) __hip_atomic_store((gu64*)(A.ws + WS_EP), tag, RLX_AGENT);
  }
  WAIT_V(0); keep(tdummy);
}

__global__ __launch_bounds__(640, 3) void fused_kernel(K1Args A) {
  extern __shared__ __attribute__((aligned(16))) char smem[];
  LAS char* lds = (LAS char*)smem;
  const int b = blockIdx.x, xcd = b & 7, idx = b >> 3, rq = xcd >> 1, ch = xcd & 1;
  const int panel = 2 * rq + (idx >> 4), c16 = idx & 15, hp = 8 * ch + (c16 >> 1), j32 = ch * 16 + c16;
  const int wid = __builtin_amdgcn_readfirstlane(threadIdx.x >> 6), lane = threadIdx.x & 63;
  if (wid >= 8) { loader(A, lds, wid == 8, lane); return; }
  if (c16 & 1) {
    if (wid < 4) k1_body<true, 0>(A, lds, panel, hp, j32, wid, lane); else k1_body<true, 1>(A, lds, panel, hp, j32, wid - 4, lane);
  } else {
    if (wid < 4) k1_body<false, 0>(A, lds, panel, hp, j32, wid, lane); else k1_body<false, 1>(A, lds, panel, hp, j32, wid - 4, lane);
  }
  BAR();
  BAR();
  k2_compute(A, lds, 4 * rq + (idx >> 3), idx & 7, ch, panel, 16 * ch + (idx & 15), wid);
}

extern "C" void kernel_launch(void* const* d_in, const int* in_sizes, int n_in, void* d_out, int out_size, void* d_ws, size_t ws_size, hipStream_t stream) {
  K1Args a; a.x = (const u16*)d_in[0]; a.wqkv = (const u16*)d_in[1]; a.w1 = (const u16*)d_in[4]; a.b1 = (const u16*)d_in[5]; a.gw = (const u16*)d_in[7]; a.ws = (char*)d_ws;
  a.w2 = (const u16*)d_in[6]; a.wo = (const u16*)d_in[2]; a.out = (u16*)d_out;
  (void)hipFuncSetAttribute((const void*)fused_kernel, hipFuncAttributeMaxDynamicSharedMemorySize, LDS_BYTES);
  fused_kernel<<<256, 640, LDS_BYTES, stream>>>(a);
}
```

```cpp
#include <hip/hip_runtime.h>
#include <stdint.h>
#include <utility>
#define ALT 3
#define K2_NA 24
#define WT0 1
#define GLATE
#define LAS __attribute__((address_space(3)))
#define DI __device__ __forceinline__
typedef unsigned short u16;
typedef short bf16x8 __attribute__((ext_vector_type(8)));
typedef short s16x4 __attribute__((ext_vector_type(4)));
typedef float f32x4 __attribute__((ext_vector_type(4)));
typedef float f32x2 __attribute__((ext_vector_type(2)));
typedef unsigned u32x4 __attribute__((ext_vector_type(4)));
typedef unsigned u32x2 __attribute__((ext_vector_type(2)));
typedef __amdgpu_buffer_rsrc_t rsrc_t;

constexpr int XA_LD = 4608;
constexpr size_t WS_XA = 0;
constexpr size_t WS_PQ = WS_XA + (size_t)2048 * XA_LD * 2;
constexpr size_t WS_KV = WS_PQ + (size_t)2048 * 1024 * 2;
constexpr size_t WS_Z  = WS_KV + (size_t)8 * 32 * 1024 * 4;
constexpr size_t WS_G  = WS_Z + (size_t)8 * 32 * 32 * 4;
constexpr size_t WS_EP    = WS_G + 2048 * 4;
constexpr size_t WS_DONE1 = WS_EP + 256;
constexpr size_t WS_ODONE = WS_DONE1 + 2048;
constexpr size_t WS_PDONE = WS_ODONE + 2048;
constexpr size_t WS_START = WS_PDONE + 128 * 128;
constexpr size_t WS_PART  = WS_START + 4096;

#define SCHED() __builtin_amdgcn_sched_barrier(0)
#define WAIT_V(n) asm volatile("s_waitcnt vmcnt(%0)" ::"n"(n) : "memory")
#define WAIT_L(n) asm volatile("s_waitcnt lgkmcnt(%0)" ::"n"(n) : "memory")
#define BAR() __builtin_amdgcn_s_barrier()

template <int OFF> DI void rd128(bf16x8& d, unsigned a) { asm volatile("ds_read_b128 %0, %1 offset:%2" : "=v"(d) : "v"(a), "i"(OFF) : "memory"); }
template <int OFF> DI void rdtr(s16x4& d, unsigned a) { asm volatile("ds_read_b64_tr_b16 %0, %1 offset:%2" : "=v"(d) : "v"(a), "i"(OFF) : "memory"); }
typedef __bf16 bf16x2_t __attribute__((ext_vector_type(2)));
DI void gload4(u32x4& d, const void* p) { asm volatile("global_load_dwordx4 %0, %1, off" : "=v"(d) : "v"(p) : "memory"); }
DI void gload1(float& d, const void* p) { asm volatile("global_load_dword %0, %1, off" : "=v"(d) : "v"(p) : "memory"); }
DI void gload2(u32x2& d, const void* p) { asm volatile("global_load_dwordx2 %0, %1, off" : "=v"(d) : "v"(p) : "memory"); }
template <class T> DI void pin(T& v) { asm volatile("" : "+v"(v)); }
template <class T> DI void keep(const T& v) { asm volatile("" :: "v"(v)); }
DI rsrc_t mk_rsrc(const void* p, unsigned n = 0x7fffffff) { return __builtin_amdgcn_make_buffer_rsrc((void*)p, 0, n, 0x00020000); }
DI void dma16(rsrc_t r, unsigned voff, unsigned soff, LAS char* l) { __builtin_amdgcn_raw_ptr_buffer_load_lds(r, (LAS void*)l, 16, voff, soff, 0, 0); }
DI unsigned cvt_pk(float lo, float hi) { unsigned r; asm("v_cvt_pk_bf16_f32 %0, %1, %2" : "=v"(r) : "v"(lo), "v"(hi)); return r; }
DI float bflo(unsigned w) { return __uint_as_float(w << 16); }
DI float bfhi(unsigned w) { return __uint_as_float(w & 0xffff0000u); }
constexpr float LOG2E = 1.44269504088896f;
DI float phi_f(float t) { const float e = __builtin_amdgcn_exp2f(__builtin_fminf(t * LOG2E, 0.f)); return e + __builtin_fmaxf(t + 1e-6f, 1e-6f); }
DI f32x2 mish2(f32x2 h) {
  const f32x2 t = h * LOG2E;
  f32x2 e; e.x = __builtin_amdgcn_exp2f(t.x); e.y = __builtin_amdgcn_exp2f(t.y);
  const f32x2 a = __builtin_elementwise_fma(e, (f32x2){0.5f, 0.5f}, (f32x2){1.f, 1.f});
  const f32x2 d = __builtin_elementwise_fma(e, a, (f32x2){1.f, 1.f});
  f32x2 r; r.x = __builtin_amdgcn_rcpf(d.x); r.y = __builtin_amdgcn_rcpf(d.y);
  return __builtin_elementwise_fma(-h, r, h);
}
DI f32x4 mish4(const f32x4& v) { const f32x2 a = mish2((f32x2){v[0], v[1]}), b = mish2((f32x2){v[2], v[3]}); return (f32x4){a.x, a.y, b.x, b.y}; }
DI f32x4 phi4(const f32x4& v) { return (f32x4){phi_f(v[0]), phi_f(v[1]), phi_f(v[2]), phi_f(v[3])}; }
#define MFMA16(a, b, c) __builtin_amdgcn_mfma_f32_16x16x32_bf16((a), (b), (c), 0, 0, 0)
#define CAT(L, H) __builtin_shufflevector((L), (H), 0, 1, 2, 3, 4, 5, 6, 7)
template <class F, int... I> DI void static_for_impl(F&& f, std::integer_sequence<int, I...>) { (f(std::integral_constant<int, I>{}), ...); }
template <int N, class F> DI void static_for(F&& f) { static_for_impl(f, std::make_integer_sequence<int, N>{}); }

constexpr int K1_XS = 32768, K1_WS = 26624, K1_WOFF = 3 * K1_XS;
constexpr int K2_XS = 16384, K2_WOFF = 98304, K2_SCR = 65536, LDS_BYTES = 163840;

struct K1Args {
  const u16* x; const u16* wqkv; const u16* w1; const u16* b1; const u16* gw; char* ws; const u16* w2; const u16* wo; u16* out;
};
typedef unsigned long long u64;
typedef __attribute__((address_space(1))) u64 gu64;
#define RLX_AGENT __ATOMIC_RELAXED, __HIP_MEMORY_SCOPE_AGENT
constexpr int SC1 = 16;
DI void touch(unsigned& dummy, const void* p) { asm volatile("global_load_dword %0, %1, off" : "+v"(dummy) : "v"(p) : "memory"); }
DI void poll_issue(u64& d, const void* p) { asm volatile("global_load_dwordx2 %0, %1, off sc1" : "=v"(d) : "v"(p) : "memory"); }
DI void wait_all(const void* p, u64 tag) {
  for (unsigned spins = 0; spins < (1u << 20); ++spins) {
    const u64 v = __hip_atomic_load((const gu64*)p, RLX_AGENT);
    if (__all(v == tag)) return;
    __builtin_amdgcn_s_sleep(1);
  }
}

template <bool KV, int WN> constexpr int blk_panel(int b) { return b == 6 ? 3 : (KV ? (b >> 1) : (b < 4 ? WN : 2)); }
template <bool KV, int WN> constexpr int blk_cb(int b) { return b == 6 ? 0 : (KV ? 2 * WN + (b & 1) : (b < 4 ? b : 2 * WN + (b & 1))); }
template <bool KV, int WN> constexpr bool blk_std(int b) { return KV && b < 4; }
template <bool KV, int WN> constexpr bool blk_bias(int b) { return KV ? b >= 4 : (WN == 1 || b >= 4); }

template <bool KV, int WN> DI void k1_epilogue(const K1Args& A, LAS char* lds, f32x4 (&acc)[4][7], int panel, int hp, int wm);
template <bool KV, int WN>
DI void k1_body(const K1Args& A, LAS char* lds, int panel, int hp, int j32, int wm, int lane) {
  constexpr int NB = 7;
  const int wid = WN * 4 + wm;
#define STAMP_AT(i)
  const int G = hp >> 1, a = hp & 1;
  const int g4 = lane >> 4, l15 = lane & 15;
  const int c0 = (KV ? 1024 : 0) + 64 * hp;
  const int c1 = KV ? (2048 + 64 * hp) : (448 * G + 128 + 128 * a);
  const int c2 = KV ? (448 * G + 64 * a) : (448 * G + 128 + 128 * a + 64);
  const int c3 = 448 * G + 384 + (KV ? 0 : 32) + 16 * a;
  auto pcol = [&](int P) { return P == 0 ? c0 : P == 1 ? c1 : P == 2 ? c2 : c3; };
  f32x4 acc[4][NB];
  static_for<NB>([&](auto B) {
    constexpr int b = B;
    f32x4 init = {0.f, 0.f, 0.f, 0.f};
    if constexpr (blk_bias<KV, WN>(b)) {
      const u32x2 w = *(const u32x2*)((const char*)A.b1 + (pcol(blk_panel<KV, WN>(b)) + 16 * blk_cb<KV, WN>(b) + 4 * g4) * 2);
      init = (f32x4){bflo(w.x), bfhi(w.x), bflo(w.y), bfhi(w.y)};
    }
#pragma unroll
    for (int mb = 0; mb < 4; ++mb) acc[mb][b] = init;
  });
  const unsigned ldsb = (unsigned)(size_t)lds;
  unsigned xcur = ldsb + (64 * wm + l15) * 128 + ((g4 ^ ((lane >> 1) & 7)) * 16);
  const int q4 = l15 >> 2, p4 = lane & 3;
  const int sr = ((q4 >> 1) & 1) | ((g4 & 1) << 1);
  unsigned aw[4];
#pragma unroll
  for (int c = 0; c < 4; ++c) aw[c] = ldsb + K1_WOFF + (8 * g4 + q4) * 128 + ((c ^ sr) * 32) + p4 * 8;
  unsigned an[2];
#pragma unroll
  for (int t = 0; t < 2; ++t) an[t] = ldsb + K1_WOFF + 24576 + (8 * g4 + ((4 * t) ^ (4 * (g4 & 1))) + q4) * 32 + p4 * 8;

  bf16x8 Af[4]; s16x4 Bl[NB], Bh[NB];
  auto readA = [&](auto MB, unsigned addr) { rd128<MB * 2048>(Af[MB], addr); };
  auto readBh = [&](auto WP, auto KS, auto B, auto H) {
    constexpr int b = B, P = blk_panel<KV, WN>(b), c = blk_cb<KV, WN>(b), wo = WP * K1_WS;
    if constexpr (P < 3) { if constexpr (H == 0) rdtr<wo + P * 8192 + KS * 4096>(Bl[b], aw[c]); else rdtr<wo + P * 8192 + KS * 4096 + 512>(Bh[b], aw[c]); }
    else { if constexpr (H == 0) rdtr<wo + KS * 1024>(Bl[b], an[0]); else rdtr<wo + KS * 1024>(Bh[b], an[1]); }
  };
  auto mma = [&](auto B, auto MB) {
    constexpr int b = B, mb = MB;
    if constexpr (blk_std<KV, WN>(b)) acc[mb][b] = MFMA16(Af[mb], CAT(Bl[b], Bh[b]), acc[mb][b]);
    else acc[mb][b] = MFMA16(CAT(Bl[b], Bh[b]), Af[mb], acc[mb][b]);
  };
  using I0 = std::integral_constant<int, 0>; using I1 = std::integral_constant<int, 1>;
  using I2 = std::integral_constant<int, 2>; using I3 = std::integral_constant<int, 3>;
  using I5 = std::integral_constant<int, 5>; using I6 = std::integral_constant<int, 6>;
  auto h1_reads = [&](auto U, auto WP, auto KS, unsigned xa_) {
    constexpr int u = U;
    if constexpr (WN == 0) {
      if constexpr (u == 0) { readA(I0{}, xa_); readBh(WP, KS, I6{}, I0{}); }
      else if constexpr (u == 1) { readA(I1{}, xa_); readBh(WP, KS, I6{}, I1{}); }
      else readBh(WP, KS, std::integral_constant<int, ((u - 2) >> 1)>{}, std::integral_constant<int, (u & 1)>{});
    } else {
      if constexpr (u == 0) readA(I0{}, xa_);
      else if constexpr (u == 1) readA(I1{}, xa_);
      else if constexpr (u < 4) readBh(WP, KS, I6{}, std::integral_constant<int, (u & 1)>{});
      else readBh(WP, KS, std::integral_constant<int, ((u - 4) >> 1)>{}, std::integral_constant<int, (u & 1)>{});
    }
  };
  constexpr int L0 = WN == 0 ? 14 : 12, L1 = 26 - L0;
  auto step = [&](auto WP, auto KS, unsigned xcur, auto WPN, auto KSN, unsigned xnext, auto BARRIER) {
    static_for<L0>([&](auto S) {
      constexpr int s = S;
      if constexpr (s == 0 || s == 2 || s == 4) WAIT_L(8);
      if constexpr (s == 6) WAIT_L(6);
      if constexpr (s == 8) WAIT_L(4);
      if constexpr (s == 10) WAIT_L(2);
      if constexpr ((s & 1) == 0) SCHED();
      if constexpr (s < 12) mma(std::integral_constant<int, (s >> 1)>{}, std::integral_constant<int, (s & 1)>{}); else mma(I6{}, std::integral_constant<int, (s & 1)>{});
      if constexpr (s == 0) readBh(WP, KS, I5{}, I0{});
      if constexpr (s == 1) readBh(WP, KS, I5{}, I1{});
      if constexpr (s == 2) readA(I2{}, xcur);
      if constexpr (s == 3) readA(I3{}, xcur);
      SCHED();
    });
    WAIT_L(0);
    if constexpr (decltype(BARRIER)::value) {
      BAR();
      if constexpr (WN == 0) __builtin_amdgcn_s_setprio(2);
    }
    else { if constexpr (WN == 0) __builtin_amdgcn_s_setprio(0); }
    SCHED();
    static_for<L1>([&](auto U) {
      constexpr int u = U;
      if constexpr (WN == 0) mma(std::integral_constant<int, (u >> 1)>{}, std::integral_constant<int, 2 + (u & 1)>{});
      else { if constexpr (u < 2) mma(I6{}, std::integral_constant<int, 2 + u>{}); else mma(std::integral_constant<int, ((u - 2) >> 1)>{}, std::integral_constant<int, 2 + (u & 1)>{}); }
      h1_reads(U, WPN, KSN, xnext);
      SCHED();
    });
  };
  STAMP_AT(1);
  BAR(); SCHED();
  STAMP_AT(2);
  static_for<L1>([&](auto U) { h1_reads(U, I0{}, I0{}, xcur); });
  if constexpr (WN == 0) __builtin_amdgcn_s_setprio(2); else __builtin_amdgcn_s_setprio(1);
  int xs = 0;
  auto ktile = [&](auto WP, int t) {
    unsigned x1 = xcur ^ 64u, xn = xcur + (xs == 2 ? (unsigned)(-2 * K1_XS) : (unsigned)K1_XS);
    pin(x1); pin(xn);
    step(WP, I0{}, xcur, WP, I1{}, x1, std::false_type{});
    step(WP, I1{}, x1, std::integral_constant<int, WP ^ 1>{}, I0{}, xn, std::true_type{});
    xcur = xn; xs = xs == 2 ? 0 : xs + 1;
  };
#pragma unroll 1
  for (int t = 0; t < 16; t += 2) { ktile(I0{}, t); ktile(I1{}, t + 1); }
  SCHED(); WAIT_L(0); SCHED();
  __builtin_amdgcn_s_setprio(0);
  STAMP_AT(3);

  k1_epilogue<KV, WN>(A, lds, acc, panel, hp, wm);
  WAIT_V(0);
}

template <bool KV, int WN>
DI void k1_epilogue(const K1Args& A, LAS char* lds, f32x4 (&acc)[4][7], int panel, int hp, int wm) {
  int lane = threadIdx.x & 63; asm volatile("" : "+v"(lane));
  const int g4 = lane >> 4, l15 = lane & 15;
  const int G = hp >> 1, a = hp & 1;
  const int c0 = (KV ? 1024 : 0) + 64 * hp;
  const int c1 = KV ? (2048 + 64 * hp) : (448 * G + 128 + 128 * a);
  const int c2 = KV ? (448 * G + 64 * a) : (448 * G + 128 + 128 * a + 64);
  const int c3 = 448 * G + 384 + (KV ? 0 : 32) + 16 * a;
  using I0 = std::integral_constant<int, 0>; using I1 = std::integral_constant<int, 1>;
  u16* xa = (u16*)(A.ws + WS_XA);
  const size_t rowbase = (size_t)panel * 256 + 64 * wm;
  LAS char* stg = lds + (WN * 4 + wm) * 8192;
  const int chunk_g = (g4 & 1) * 2 + (g4 >> 1);
  auto store_line = [&](auto ACT, int col0) {
    LAS char* tile = ACT == 0 ? lds + K2_SCR + wm * 8192 : stg;
    static_for<4>([&](auto MB) {
      constexpr int mb = MB;
      static_for<2>([&](auto PP) {
        constexpr int p = PP;
        f32x4 e = acc[mb][2 * p], o = acc[mb][2 * p + 1];
        if constexpr (ACT == 0) { e = phi4(e); o = phi4(o); } else { e = mish4(e); o = mish4(o); }
        const unsigned e0 = cvt_pk(e[0], e[1]), e1 = cvt_pk(e[2], e[3]), o0 = cvt_pk(o[0], o[1]), o1 = cvt_pk(o[2], o[3]);
        const auto r0 = __builtin_amdgcn_permlane16_swap(e0, o0, false, false);
        const auto r1 = __builtin_amdgcn_permlane16_swap(e1, o1, false, false);
        const u32x4 w = {r0[0], r1[0], r0[1], r1[1]};
        *(LAS u32x4*)(tile + (16 * mb + l15) * 128 + (((4 * p + chunk_g) ^ (l15 & 7)) * 16)) = w;
      });
      if constexpr (ACT == 1) {
#pragma unroll
        for (int i = 2 * mb; i < 2 * mb + 2; ++i) {
          const u32x4 w = *(LAS u32x4*)(tile + i * 1024 + lane * 16);
          const int r = 8 * i + (lane >> 3), ch = (lane & 7) ^ (r & 7);
          __builtin_amdgcn_raw_buffer_store_b128(w, mk_rsrc(xa), (unsigned)(((rowbase + r) * XA_LD + col0 + ch * 8) * 2), 0, SC1);
        }
      }
    });
  };
  auto store_half = [&](int col0) {
#pragma unroll
    for (int mb = 0; mb < 4; ++mb) {
      const f32x4 e = mish4(acc[mb][4]), o = mish4(acc[mb][5]);
      const unsigned e0 = cvt_pk(e[0], e[1]), e1 = cvt_pk(e[2], e[3]), o0 = cvt_pk(o[0], o[1]), o1 = cvt_pk(o[2], o[3]);
      const auto r0 = __builtin_amdgcn_permlane16_swap(e0, o0, false, false);
      const auto r1 = __builtin_amdgcn_permlane16_swap(e1, o1, false, false);
      const u32x4 w = {r0[0], r1[0], r0[1], r1[1]};
      __builtin_amdgcn_raw_buffer_store_b128(w, mk_rsrc(xa), (unsigned)(((rowbase + 16 * mb + l15) * XA_LD + col0 + chunk_g * 8) * 2), 0, SC1);
    }
  };
  auto store_narrow = [&]() {
#pragma unroll
    for (int m2 = 0; m2 < 2; ++m2) {
      const int mb = 2 * WN + m2;
      const f32x4 v = mish4(acc[2 * WN + m2][6]);
      const u32x2 w = {cvt_pk(v[0], v[1]), cvt_pk(v[2], v[3])};
      __builtin_amdgcn_raw_buffer_store_b64(w, mk_rsrc(xa), (unsigned)(((rowbase + 16 * mb + l15) * XA_LD + c3 + 4 * g4) * 2), 0, SC1);
    }
  };
  if constexpr (!KV && WN == 1) store_line(I1{}, c1);
  store_half(c2 + 32 * WN);
  store_narrow();
  if constexpr (!KV) {
    if constexpr (WN == 0) store_line(I0{}, c0);
  } else {
    f32x4 kvD[4]; f32x4 zD[2];
#pragma unroll
    for (int i = 0; i < 4; ++i) kvD[i] = (f32x4){0.f, 0.f, 0.f, 0.f};
    zD[0] = zD[1] = (f32x4){0.f, 0.f, 0.f, 0.f};
    const bf16x8 ones = {0x3f80, 0x3f80, 0x3f80, 0x3f80, 0x3f80, 0x3f80, 0x3f80, 0x3f80};
#pragma unroll
    for (int mp = 0; mp < 2; ++mp) {
      bf16x8 ka[2], vb[2];
#pragma unroll
      for (int b = 0; b < 2; ++b) {
        const f32x4 k0 = phi4(acc[2 * mp][b]), k1 = phi4(acc[2 * mp + 1][b]);
        const u32x4 w = {cvt_pk(k0[0], k0[1]), cvt_pk(k0[2], k0[3]), cvt_pk(k1[0], k1[1]), cvt_pk(k1[2], k1[3])};
        ka[b] = __builtin_bit_cast(bf16x8, w);
        const f32x4 v0 = acc[2 * mp][2 + b], v1 = acc[2 * mp + 1][2 + b];
        const u32x4 u = {cvt_pk(v0[0], v0[1]), cvt_pk(v0[2], v0[3]), cvt_pk(v1[0], v1[1]), cvt_pk(v1[2], v1[3])};
        vb[b] = __builtin_bit_cast(bf16x8, u);
      }
#pragma unroll
      for (int db = 0; db < 2; ++db) {
#pragma unroll
        for (int eb = 0; eb < 2; ++eb) kvD[db * 2 + eb] = MFMA16(ka[db], vb[eb], kvD[db * 2 + eb]);
        zD[db] = MFMA16(ka[db], ones, zD[db]);
      }
    }
    LAS char* sc = lds + 2 * K1_XS;
    LAS char* sz = lds;
#pragma unroll
    for (int i = 0; i < 4; ++i) *(LAS f32x4*)(sc + ((WN * 4 + wm) * 4 + i) * 1024 + lane * 16) = kvD[i];
    if (l15 == 0) {
#pragma unroll
      for (int db = 0; db < 2; ++db) *(LAS f32x4*)(sz + ((WN * 4 + wm) * 2 + db) * 64 + g4 * 16) = zD[db];
    }
    WAIT_L(0); BAR(); SCHED();
    const int blk = wm;
    f32x4 s = {0.f, 0.f, 0.f, 0.f};
#pragma unroll
    for (int m = 0; m < 4; ++m) s += *(LAS f32x4*)(sc + ((WN * 4 + m) * 4 + blk) * 1024 + lane * 16);
    __builtin_amdgcn_raw_buffer_store_b128(__builtin_bit_cast(u32x4, s), mk_rsrc(A.ws + WS_KV),
        (unsigned)(((panel * 32 + 2 * hp + WN) * 1024 + (16 * (blk & 1) + l15) * 32 + 16 * (blk >> 1) + 4 * g4) * 4), 0, SC1);
    if (wm == 0 && lane < 8) {
      const int db = lane >> 2, gq = lane & 3;
      f32x4 zs = {0.f, 0.f, 0.f, 0.f};
#pragma unroll
      for (int m = 0; m < 4; ++m) zs += *(LAS f32x4*)(sz + ((WN * 4 + m) * 2 + db) * 64 + gq * 16);
      __builtin_amdgcn_raw_buffer_store_b128(__builtin_bit_cast(u32x4, zs), mk_rsrc(A.ws + WS_Z), (unsigned)(((panel * 32 + 2 * hp + WN) * 32 + 16 * db + 4 * gq) * 4), 0, SC1);
    }
  }
}

DI void k2_compute(const K1Args& A, LAS char* lds, int tm, int tn, int role, int panel, bool isq, int hp, int wid) {
  int lane = threadIdx.x & 63; asm volatile("" : "+v"(lane));
  const int nt = role == 0 ? K2_NA + 16 : 56 - K2_NA, tile = tm * 8 + tn;
  const int wm = wid >> 2, wn = wid & 3;
  const int g4 = lane >> 4, l15 = lane & 15;
  const unsigned prow = (unsigned)panel * 256 + 32 * wid;
  const rsrc_t rws = mk_rsrc(A.ws);
  u32x4 part[8], zq; bf16x8 pf[2][2]; unsigned gv[2], gq[4];
  if (isq) {
#pragma unroll
    for (int q = 0; q < 8; ++q) part[q] = __builtin_amdgcn_raw_buffer_load_b128(rws, (unsigned)(WS_KV + ((q * 32 + 2 * hp) * 1024 + (wid * 64 + lane) * 4) * 4), 0, SC1);
    zq = __builtin_amdgcn_raw_buffer_load_b128(rws, (unsigned)(WS_Z + ((lane >> 3) & 7) * 4096 + (2 * hp * 32 + (wid & 1) * 32 + (lane & 7) * 4) * 4), 0, SC1);
#pragma unroll
    for (int mb = 0; mb < 2; ++mb) {
      const int r = 32 * wid + 16 * mb + l15;
#pragma unroll
      for (int h = 0; h < 2; ++h) pf[mb][h] = *(LAS bf16x8*)(lds + K2_SCR + r * 128 + (((4 * h + g4) ^ (r & 7)) * 16));
      gv[mb] = __builtin_amdgcn_raw_buffer_load_b32(rws, (unsigned)(WS_G + (prow + 16 * mb + l15) * 4), 0, SC1);
    }
  }
#pragma unroll
  for (int mb = 0; mb < 4; ++mb) gq[mb] = __builtin_amdgcn_raw_buffer_load_b32(rws, (unsigned)(WS_G + (tm * 128 + 64 * wm + 16 * mb + l15) * 4), 0, SC1);
  const unsigned ldsb = (unsigned)(size_t)lds;
  unsigned ax[2];
#pragma unroll
  for (int ks = 0; ks < 2; ++ks) ax[ks] = ldsb + (64 * wm + l15) * 128 + (((4 * ks + g4) ^ ((lane >> 1) & 7)) * 16);
  const int q4 = l15 >> 2, p4 = lane & 3;
  const int sr = ((q4 >> 1) & 1) | ((g4 & 1) << 1);
  unsigned aw[2];
#pragma unroll
  for (int nb = 0; nb < 2; ++nb) aw[nb] = ldsb + K2_WOFF + (wn >> 1) * 8192 + (8 * g4 + q4) * 128 + (((2 * (wn & 1) + nb) ^ sr) * 32) + p4 * 8;
  f32x4 acc[4][2];
#pragma unroll
  for (int i = 0; i < 4; ++i) acc[i][0] = acc[i][1] = (f32x4){0.f, 0.f, 0.f, 0.f};
  bf16x8 Af[2][2][4]; s16x4 Bl[2][2][2], Bh[2][2][2];
  auto rd = [&](auto BUF, auto I, const unsigned (&pa)[2], const unsigned (&pw)[2]) {
    constexpr int i = I, ks = i >> 3, r = i & 7;
    if constexpr (r < 2) { if constexpr (r == 0) rdtr<ks * 4096>(Bl[BUF][ks][0], pw[0]); else rdtr<ks * 4096 + 512>(Bh[BUF][ks][0], pw[0]); }
    else if constexpr (r < 6) rd128<(r - 2) * 2048>(Af[BUF][ks][r - 2], pa[ks]);
    else { if constexpr (r == 6) rdtr<ks * 4096>(Bl[BUF][ks][1], pw[1]); else rdtr<ks * 4096 + 512>(Bh[BUF][ks][1], pw[1]); }
  };
  using I0 = std::integral_constant<int, 0>; using I1 = std::integral_constant<int, 1>;
  LAS char* kvs = lds + K2_SCR;
  f32x4 ksum = {0.f, 0.f, 0.f, 0.f}, zsum = {0.f, 0.f, 0.f, 0.f};
  if (isq) {
#pragma unroll
    for (int q = 0; q < 8; ++q) ksum += __builtin_bit_cast(f32x4, part[q]);
    zsum = __builtin_bit_cast(f32x4, zq);
    if (wid < 2) {
#pragma unroll
      for (int o = 8; o <= 32; o <<= 1) { zsum[0] += __shfl_xor(zsum[0], o); zsum[1] += __shfl_xor(zsum[1], o); zsum[2] += __shfl_xor(zsum[2], o); zsum[3] += __shfl_xor(zsum[3], o); }
    }
    pin(pf[0][0]); pin(pf[0][1]); pin(pf[1][0]); pin(pf[1][1]);
  }
  WAIT_L(0);
  STAMP_AT(1);
  BAR(); SCHED();
  STAMP_AT(2);
  if (isq) {
    *(LAS u32x2*)(kvs + (wid * 64 + lane) * 8) = (u32x2){cvt_pk(ksum[0], ksum[1]), cvt_pk(ksum[2], ksum[3])};
    if (wid < 2 && lane < 8) *(LAS u32x2*)(kvs + 4096 + wid * 64 + lane * 8) = (u32x2){cvt_pk(zsum[0], zsum[1]), cvt_pk(zsum[2], zsum[3])};
  }
  SCHED();
  static_for<16>([&](auto I) { rd(I0{}, I, ax, aw); });
  auto body = [&](auto CUR, int i) {
    constexpr int cur = CUR;
    const unsigned so1 = ((i + 1) & 3) * K2_XS;
    unsigned pa[2] = {ax[0] + so1, ax[1] + so1}, pw[2] = {aw[0] + so1, aw[1] + so1};
    pin(pa[0]); pin(pa[1]); pin(pw[0]); pin(pw[1]);
    WAIT_L(0); BAR(); SCHED();
    static_for<16>([&](auto S) {
      constexpr int s = S, ks = s >> 3, nb = (s >> 2) & 1, mb = s & 3;
      acc[mb][nb] = MFMA16(CAT(Bl[cur][ks][nb], Bh[cur][ks][nb]), Af[cur][ks][mb], acc[mb][nb]);
      if constexpr (s < 8) { rd(std::integral_constant<int, cur ^ 1>{}, S, pa, pw); }
      else if constexpr (s < 12) { rd(std::integral_constant<int, cur ^ 1>{}, std::integral_constant<int, 2 * s - 8>{}, pa, pw); rd(std::integral_constant<int, cur ^ 1>{}, std::integral_constant<int, 2 * s - 7>{}, pa, pw); }
      SCHED();
    });
  };
  body(I0{}, 0);
  if (isq) {
    const unsigned kb = (unsigned)(size_t)kvs;
    const int chunk_g = (g4 & 1) * 2 + (g4 >> 1);
#pragma unroll
    for (int h = 0; h < 2; ++h) {
      bf16x8 ka[2], za;
      rd128<0>(ka[0], kb + h * 2048 + l15 * 64 + g4 * 16); rd128<1024>(ka[1], kb + h * 2048 + l15 * 64 + g4 * 16); rd128<4096>(za, kb + h * 64 + g4 * 16);
      WAIT_L(0); SCHED();
#pragma unroll
      for (int mb = 0; mb < 2; ++mb) {
        const f32x4 zero = {0.f, 0.f, 0.f, 0.f};
        const f32x4 n0 = MFMA16(ka[0], pf[mb][h], zero), n1 = MFMA16(ka[1], pf[mb][h], zero), dn = MFMA16(za, pf[mb][h], zero);
        const float sc = __uint_as_float(gv[mb]) * __builtin_amdgcn_rcpf(dn[0] + 1e-6f);
        const f32x4 e = n0 * sc, o = n1 * sc;
        const unsigned e0 = cvt_pk(e[0], e[1]), e1 = cvt_pk(e[2], e[3]), o0 = cvt_pk(o[0], o[1]), o1 = cvt_pk(o[2], o[3]);
        const auto r0 = __builtin_amdgcn_permlane16_swap(e0, o0, false, false);
        const auto r1 = __builtin_amdgcn_permlane16_swap(e1, o1, false, false);
        const u32x4 w = {r0[0], r1[0], r0[1], r1[1]};
        __builtin_amdgcn_raw_buffer_store_b128(w, rws, (unsigned)(WS_XA + ((prow + 16 * mb + l15) * XA_LD + 3584 + 64 * hp + 32 * h + chunk_g * 8) * 2), 0, SC1);
      }
    }
    SCHED();
  }
  body(I1{}, 1);
  body(I0{}, 2);
  float omg[4];
#pragma unroll
  for (int mb = 0; mb < 4; ++mb) omg[mb] = 1.f - __uint_as_float(gq[mb]);
  body(I1{}, 3);
  WAIT_V(0);
  WAIT_L(0); SCHED();
  const int n1 = role == 0 ? K2_NA : nt;
#pragma unroll 1
  for (int i = 4; i < n1; i += 2) { body(I0{}, i); body(I1{}, i + 1); }
  SCHED(); WAIT_L(0); SCHED();
#pragma unroll
  for (int mb = 0; mb < 4; ++mb) { acc[mb][0] *= omg[mb]; acc[mb][1] *= omg[mb]; }
#pragma unroll 1
  for (int i = n1; i < nt; i += 2) { body(I0{}, i); body(I1{}, i + 1); }
  SCHED(); WAIT_L(0); SCHED();
  STAMP_AT(3);
  if (role == 1) {
    const unsigned poff = (unsigned)(WS_PART + (tile * 8 + wid) * 4096 + lane * 16);
#pragma unroll
    for (int mb = 0; mb < 4; ++mb) {
      const u32x4 w = {cvt_pk(acc[mb][0][0], acc[mb][0][1]), cvt_pk(acc[mb][0][2], acc[mb][0][3]), cvt_pk(acc[mb][1][0], acc[mb][1][1]), cvt_pk(acc[mb][1][2], acc[mb][1][3])};
      __builtin_amdgcn_raw_buffer_store_b128(w, rws, poff + mb * 1024, 0, SC1);
    }
    STAMP_AT(4);
    WAIT_V(0);
    BAR();
  } else {
    BAR();
    BAR();
    STAMP_AT(4);
    const int chunk_g = (g4 & 1) * 2 + (g4 >> 1);
    LAS char* pl = lds + K2_SCR + wid * 4096 + lane * 16;
#pragma unroll
    for (int mb = 0; mb < 4; ++mb) {
      const u32x4 pw_ = *(LAS u32x4*)(pl + mb * 1024);
      const f32x4 e = acc[mb][0] + (f32x4){bflo(pw_[0]), bfhi(pw_[0]), bflo(pw_[1]), bfhi(pw_[1])}, o = acc[mb][1] + (f32x4){bflo(pw_[2]), bfhi(pw_[2]), bflo(pw_[3]), bfhi(pw_[3])};
      const unsigned e0 = cvt_pk(e[0], e[1]), e1 = cvt_pk(e[2], e[3]), o0 = cvt_pk(o[0], o[1]), o1 = cvt_pk(o[2], o[3]);
      const auto r0 = __builtin_amdgcn_permlane16_swap(e0, o0, false, false);
      const auto r1 = __builtin_amdgcn_permlane16_swap(e1, o1, false, false);
      const u32x4 w = {r0[0], r1[0], r0[1], r1[1]};
      *(u32x4*)(A.out + (size_t)(tm * 128 + 64 * wm + 16 * mb + l15) * 1024 + tn * 128 + 32 * wn + chunk_g * 8) = w;
    }
  }
}

DI void loader(const K1Args& A, LAS char* lds, bool isx, int lane) {
  const int b = blockIdx.x, xcd = b & 7, idx = b >> 3, rq = xcd >> 1, ch = xcd & 1;
  const int panel = 2 * rq + (idx >> 4), c16 = idx & 15, hp = 8 * ch + (c16 >> 1), j32 = ch * 16 + c16;
  const bool kv = (c16 & 1) != 0;
  u64 ep; poll_issue(ep, A.ws + WS_EP);
  const int G = hp >> 1, a = hp & 1;
  const int c0 = (kv ? 1024 : 0) + 64 * hp;
  const int c1 = kv ? (2048 + 64 * hp) : (448 * G + 128 + 128 * a);
  const int c2 = kv ? (448 * G + 64 * a) : (448 * G + 128 + 128 * a + 64);
  const int c3 = 448 * G + 384 + (kv ? 0 : 32) + 16 * a;
  unsigned chx[2], chw[2];
#pragma unroll
  for (int par = 0; par < 2; ++par) {
    chx[par] = (unsigned)(((lane & 7) ^ ((4 * par + (lane >> 4)) & 7)) * 16);
    chw[par] = (unsigned)(((lane & 7) ^ ((((lane >> 4) & 1) | (par << 1)) << 1)) * 16);
  }
  unsigned tdummy = 0;
  u64 tag;
  {
    unsigned vA[4], vB[4], vN[2];
#pragma unroll
    for (int k = 0; k < 4; ++k) {
      vA[k] = (isx ? (unsigned)((8 * k + (lane >> 3)) * 2048) + chx[k & 1] : (unsigned)((8 * k + (lane >> 3)) * 6144) + chw[k & 1]) + 3072 - k * 1024;
      vB[k] = (unsigned)((8 * k + (lane >> 3)) * 7168) + chw[k & 1] + 3072 - k * 1024;
    }
#pragma unroll
    for (int n = 0; n < 2; ++n) { const int rho = 32 * n + (lane >> 1); vN[n] = (unsigned)((rho ^ (((rho >> 3) & 1) << 2)) * 7168 + (lane & 1) * 16) + 3072 - n * 1024; }
    const rsrc_t rx = mk_rsrc((const char*)A.x - 3072), rqk = mk_rsrc((const char*)A.wqkv - 3072), r1 = mk_rsrc((const char*)A.w1 - 3072);
    const unsigned xrow0 = (unsigned)panel * 256 * 2048;
    auto issueX = [&](int t, int xs) {
#pragma unroll
      for (int g = 0; g < 8; ++g)
        static_for<4>([&](auto K) { __builtin_amdgcn_raw_ptr_buffer_load_lds(rx, (LAS void*)(lds + xs * K1_XS + g * 4096), 16, vA[K], xrow0 + t * 128 + g * 65536, K * 1024, 0); });
    };
    auto issueW = [&](int t) {
      LAS char* dst = lds + K1_WOFF + (t & 1) * K1_WS;
#pragma unroll
      for (int g = 0; g < 2; ++g)
        static_for<4>([&](auto K) { __builtin_amdgcn_raw_ptr_buffer_load_lds(rqk, (LAS void*)(dst + g * 4096), 16, vA[K], (t * 64 + g * 32) * 6144 + c0 * 2, K * 1024, 0); });
      if (kv) {
#pragma unroll
        for (int g = 0; g < 2; ++g)
          static_for<4>([&](auto K) { __builtin_amdgcn_raw_ptr_buffer_load_lds(rqk, (LAS void*)(dst + 8192 + g * 4096), 16, vA[K], (t * 64 + g * 32) * 6144 + c1 * 2, K * 1024, 0); });
      } else {
#pragma unroll
        for (int g = 0; g < 2; ++g)
          static_for<4>([&](auto K) { __builtin_amdgcn_raw_ptr_buffer_load_lds(r1, (LAS void*)(dst + 8192 + g * 4096), 16, vB[K], (t * 64 + g * 32) * 7168 + c1 * 2, K * 1024, 0); });
      }
#pragma unroll
      for (int g = 0; g < 2; ++g)
        static_for<4>([&](auto K) { __builtin_amdgcn_raw_ptr_buffer_load_lds(r1, (LAS void*)(dst + 16384 + g * 4096), 16, vB[K], (t * 64 + g * 32) * 7168 + c2 * 2, K * 1024, 0); });
      static_for<2>([&](auto N) { __builtin_amdgcn_raw_ptr_buffer_load_lds(r1, (LAS void*)(dst + 24576), 16, vN[N], t * 64 * 7168 + c3 * 2, N * 1024, 0); });
    };
    auto touch3 = [&]() {
      const int w = b * 2 + (isx ? 0 : 1);
#pragma unroll
      for (int n = 0; n < 3; ++n) {
        const int k = n * 64 + lane, li = w * 144 + (k < 144 ? k : 143);
        touch(tdummy, li < 57344 ? (const char*)A.w2 + (size_t)li * 128 : (const char*)A.wo + (size_t)(li - 57344) * 128);
      }
    };
    if (isx) {
      issueX(0, 0); issueX(1, 1);
#pragma unroll
      for (int n = 0; n < 4; ++n) touch(tdummy, (const char*)A.x + (size_t)(panel * 256 + 16 * c16 + 4 * n + (lane >> 4)) * 2048 + (lane & 15) * 128);
      auto wtouch = [&](int t0) {
        const int li = b * 52 + (lane < 52 ? lane : 51);
        int row = 64 * t0 + li / 104; row = row < 1024 ? row : 1023;
        const int c = li % 104;
        touch(tdummy, c < 48 ? (const char*)A.wqkv + (size_t)row * 6144 + c * 128 : (const char*)A.w1 + (size_t)row * 7168 + (c - 48) * 128);
      };
#pragma unroll
      for (int n = 0; n < WT0; ++n) wtouch(2 + 2 * n);
      WAIT_V(36 + WT0); pin(ep); tag = ep + 1;
      BAR();
      if (lane == 0) __hip_atomic_store((gu64*)(A.ws + WS_START) + b, tag, RLX_AGENT);
      issueX(2, 2);
      int xs = 0;
      auto xiter = [&](int t) {
        WAIT_V(32); BAR(); issueX(t + 3, xs); xs = xs == 2 ? 0 : xs + 1;
        if (2 * (t + WT0) + 2 < 16) wtouch(2 * (t + WT0) + 2);
      };
#pragma unroll 1
      for (int t = 0; t < 8; ++t) xiter(t);
      xiter(8); xiter(9);
#pragma unroll 1
      for (int t = 10; t < 13; ++t) xiter(t);
      touch3();
      WAIT_V(35); BAR();
      WAIT_V(3); BAR();
      u32x4 gx[8][2], gg[2];
#pragma unroll
      for (int r = 0; r < 8; ++r) { const char* xr = (const char*)A.x + (size_t)(panel * 256 + j32 * 8 + r) * 2048 + lane * 32; gload4(gx[r][0], xr); gload4(gx[r][1], xr + 16); }
      gload4(gg[0], (const char*)A.gw + lane * 32); gload4(gg[1], (const char*)A.gw + lane * 32 + 16);
      BAR();
      WAIT_V(0);
      {
        pin(gg[0]); pin(gg[1]);
        float s[8];
#pragma unroll
        for (int r = 0; r < 8; ++r) {
          pin(gx[r][0]); pin(gx[r][1]);
          s[r] = 0.f;
#pragma unroll
          for (int h = 0; h < 2; ++h)
#pragma unroll
            for (int i = 0; i < 4; ++i) { const unsigned ux = gx[r][h][i], ug = gg[h][i]; s[r] = __builtin_amdgcn_fdot2_f32_bf16(__builtin_bit_cast(bf16x2_t, ux), __builtin_bit_cast(bf16x2_t, ug), s[r], false); }
        }
#pragma unroll
        for (int o = 32; o >= 1; o >>= 1)
#pragma unroll
          for (int r = 0; r < 8; ++r) s[r] += __shfl_xor(s[r], o);
        float mine = s[0];
#pragma unroll
        for (int r = 1; r < 8; ++r) mine = lane == r ? s[r] : mine;
        const float gval = __builtin_amdgcn_rcpf(1.f + __builtin_amdgcn_exp2f(-LOG2E * mine));
        if (lane < 8) __hip_atomic_store((__attribute__((address_space(1))) unsigned*)(A.ws + WS_G) + panel * 256 + j32 * 8 + lane, __float_as_uint(gval), RLX_AGENT);
      }
    } else {
      issueW(0); issueW(1);
      WAIT_V(26); pin(ep); tag = ep + 1;
      BAR();
#pragma unroll 1
      for (int t = 0; t < 14; ++t) { WAIT_V(0); BAR(); issueW(t + 2); }
      touch3();
      WAIT_V(3); BAR();
      BAR();
    }
  }
  const int role = (idx >> 1) & 1, tm = 4 * rq + (idx >> 3), tn = 4 * ch + 2 * ((idx >> 2) & 1) + (idx & 1), tile = tm * 8 + tn;
  const int nt = role == 0 ? K2_NA + 16 : 56 - K2_NA;
  unsigned voff[4];
#pragma unroll
  for (int k = 0; k < 4; ++k)
    voff[k] = (isx ? (unsigned)((8 * k + (lane >> 3)) * (XA_LD * 2) + chx[k & 1]) : (unsigned)((8 * k + (lane >> 3)) * 2048 + chw[k & 1])) + 3072 - k * 1024;
  const unsigned gstep = isx ? 32 * XA_LD * 2 : 32 * 2048;
  auto issue = [&](rsrc_t r, unsigned so, int slot) {
    LAS char* st = lds + (isx ? 0 : K2_WOFF) + slot * K2_XS;
#pragma unroll
    for (int g = 0; g < 4; ++g) {
      const unsigned sg = isx ? so + g * gstep : so + (g & 1) * gstep + (g >> 1) * 128;
      static_for<4>([&](auto K) { __builtin_amdgcn_raw_ptr_buffer_load_lds(r, (LAS void*)(st + g * 4096), 16, voff[K], sg, K * 1024, SC1); });
    }
  };
  const rsrc_t r0 = mk_rsrc(isx ? (const char*)(A.ws + WS_XA) - 3072 : (const char*)A.w2 - 3072);
  const rsrc_t r1 = mk_rsrc(isx ? (const char*)(A.ws + WS_XA) - 3072 : (const char*)A.wo - 3072);
  const unsigned tstep = isx ? 128u : 64u * 2048u;
  const unsigned sbase = isx ? (unsigned)tm * 128 * (XA_LD * 2) : (unsigned)tn * 256;
  const unsigned s0 = sbase + (role == 0 ? 0 : K2_NA) * tstep;
  const unsigned s1 = isx ? sbase + 56 * tstep : sbase;
  const rsrc_t rp = mk_rsrc(A.ws + WS_PART);
  auto issue_partial = [&]() {
    LAS char* dst = lds + K2_SCR + (isx ? 0 : 16384);
#pragma unroll
    for (int g = 0; g < 4; ++g)
      static_for<4>([&](auto K) { __builtin_amdgcn_raw_ptr_buffer_load_lds(rp, (LAS void*)(dst + g * 4096), 16, lane * 16, (unsigned)tile * 32768 + (isx ? 0 : 16384) + g * 4096, K * 1024, SC1); });
  };
  if (!isx) { issue(r0, s0, 0); issue(r0, s0 + tstep, 1); issue(r0, s0 + 2 * tstep, 2); issue(r0, s0 + 3 * tstep, 3); }
  if (kv) BAR();
  if (isx) WAIT_V(0);
  BAR();
  if (isx) {
    if (lane == 0) __hip_atomic_store((gu64*)(A.ws + WS_DONE1) + panel * 32 + j32, tag, RLX_AGENT);
    const int slot = lane < 32 ? panel * 32 + lane : ((lane < 40 && !kv) ? (lane - 32) * 32 + (j32 | 1) : panel * 32);
    wait_all(A.ws + WS_DONE1 + (size_t)slot * 8, tag);
  }
  BAR();
  if (isx) { issue(r0, s0, 0); issue(r0, s0 + tstep, 1); issue(r0, s0 + 2 * tstep, 2); WAIT_V(32); } else WAIT_V(48);
  BAR();
  if (isx) issue(r0, s0 + 3 * tstep, 3);
  auto run = [&](int i0, int i1, rsrc_t r, unsigned so) {
#pragma unroll 1
    for (int i = i0; i < i1; ++i, so += tstep) { WAIT_V(32); BAR(); issue(r, so, i & 3); }
  };
  u64 pv = 0;
  run(0, 5, r0, s0 + 4 * tstep);
  if (isx && !kv && lane == 0) __hip_atomic_store((gu64*)(A.ws + WS_ODONE) + panel * 16 + hp, tag, RLX_AGENT);
  if (role == 0) {
    run(5, K2_NA - 8, r0, s0 + 9 * tstep);
    const char* od = A.ws + WS_ODONE + (size_t)(panel * 16 + (lane & 15)) * 8;
    if (isx) poll_issue(pv, (const void*)od);
    run(K2_NA - 8, K2_NA - 4, r0, s0 + (K2_NA - 4) * tstep);
    if (isx) { WAIT_V(32); pin(pv); if (!__all(pv == tag)) wait_all((const void*)od, tag); }
    run(K2_NA - 4, nt - 4, r1, s1);
  } else run(5, nt - 4, r0, s0 + 9 * tstep);
  const char* pd = A.ws + WS_PDONE + (size_t)tile * 128;
  poll_issue(pv, (const void*)pd);
  WAIT_V(33); BAR();
  WAIT_V(17); BAR();
  WAIT_V(0);
  bool early = false;
  if (role == 0) { pin(pv); early = __all(pv == tag); }
  BAR();
  if (early) issue_partial();
  BAR();
  if (role == 0) {
    if (!early) wait_all((const void*)pd, tag);
    BAR();
    if (!early) issue_partial();
    WAIT_V(0);
    BAR();
  } else {
    BAR();
    if (isx && lane == 0) __hip_atomic_store((gu64*)(A.ws + WS_PDONE + (size_t)tile * 128), tag, RLX_AGENT);
  }
  if (b == 0 && isx) {
#pragma unroll 1
    for (int k = 0; k < 4; ++k) wait_all(A.ws + WS_START + (size_t)(k * 64 + lane) * 8, tag);
    if (lane == 0) __hip_atomic_store((gu64*)(A.ws + WS_EP), tag, RLX_AGENT);
  }
  WAIT_V(0); keep(tdummy);
}

__global__ __launch_bounds__(640, 3) void fused_kernel(K1Args A) {
  extern __shared__ __attribute__((aligned(16))) char smem[];
  LAS char* lds = (LAS char*)smem;
  const int b = blockIdx.x, xcd = b & 7, idx = b >> 3, rq = xcd >> 1, ch = xcd & 1;
  const int panel = 2 * rq + (idx >> 4), c16 = idx & 15, hp = 8 * ch + (c16 >> 1), j32 = ch * 16 + c16;
  const int wid = __builtin_amdgcn_readfirstlane(threadIdx.x >> 6), lane = threadIdx.x & 63;
  if (wid >= 8) { loader(A, lds, wid == 8, lane); return; }
  if (c16 & 1) {
    if (wid < 4) k1_body<true, 0>(A, lds, panel, hp, j32, wid, lane); else k1_body<true, 1>(A, lds, panel, hp, j32, wid - 4, lane);
  } else {
    if (wid < 4) k1_body<false, 0>(A, lds, panel, hp, j32, wid, lane); else k1_body<false, 1>(A, lds, panel, hp, j32, wid - 4, lane);
  }
  BAR();
  BAR();
  k2_compute(A, lds, 4 * rq + (idx >> 3), 4 * ch + 2 * ((idx >> 2) & 1) + (idx & 1), (idx >> 1) & 1, panel, (c16 & 1) == 0, hp, wid);
}

extern "C" void kernel_launch(void* const* d_in, const int* in_sizes, int n_in, void* d_out, int out_size, void* d_ws, size_t ws_size, hipStream_t stream) {
  K1Args a; a.x = (const u16*)d_in[0]; a.wqkv = (const u16*)d_in[1]; a.w1 = (const u16*)d_in[4]; a.b1 = (const u16*)d_in[5]; a.gw = (const u16*)d_in[7]; a.ws = (char*)d_ws;
  a.w2 = (const u16*)d_in[6]; a.wo = (const u16*)d_in[2]; a.out = (u16*)d_out;
  (void)hipFuncSetAttribute((const void*)fused_kernel, hipFuncAttributeMaxDynamicSharedMemorySize, LDS_BYTES);
  fused_kernel<<<256, 640, LDS_BYTES, stream>>>(a);
}
```

```cpp
#include <hip/hip_runtime.h>
#include <stdint.h>
#include <utility>
#define ALT 3
#define K2_NA 24
#define LAS __attribute__((address_space(3)))
#define DI __device__ __forceinline__
typedef unsigned short u16;
typedef short bf16x8 __attribute__((ext_vector_type(8)));
typedef short s16x4 __attribute__((ext_vector_type(4)));
typedef float f32x4 __attribute__((ext_vector_type(4)));
typedef float f32x2 __attribute__((ext_vector_type(2)));
typedef unsigned u32x4 __attribute__((ext_vector_type(4)));
typedef unsigned u32x2 __attribute__((ext_vector_type(2)));
typedef __amdgpu_buffer_rsrc_t rsrc_t;

constexpr int XA_LD = 4608;
constexpr size_t WS_XA = 0;
constexpr size_t WS_PQ = WS_XA + (size_t)2048 * XA_LD * 2;
constexpr size_t WS_KV = WS_PQ + (size_t)2048 * 1024 * 2;
constexpr size_t WS_Z  = WS_KV + (size_t)8 * 32 * 1024 * 4;
constexpr size_t WS_G  = WS_Z + (size_t)8 * 32 * 32 * 4;
constexpr size_t WS_EP    = WS_G + 2048 * 4;
constexpr size_t WS_DONE1 = WS_EP + 256;
constexpr size_t WS_ODONE = WS_DONE1 + 2048;
constexpr size_t WS_PDONE = WS_ODONE + 2048;
constexpr size_t WS_START = WS_PDONE + 128 * 128;
constexpr size_t WS_PART  = WS_START + 4096;
constexpr size_t WS_NB    = WS_PART + 128 * 32768;

#define SCHED() __builtin_amdgcn_sched_barrier(0)
#define WAIT_V(n) asm volatile("s_waitcnt vmcnt(%0)" ::"n"(n) : "memory")
#define WAIT_L(n) asm volatile("s_waitcnt lgkmcnt(%0)" ::"n"(n) : "memory")
#define BAR() __builtin_amdgcn_s_barrier()

template <int OFF> DI void rd128(bf16x8& d, unsigned a) { asm volatile("ds_read_b128 %0, %1 offset:%2" : "=v"(d) : "v"(a), "i"(OFF) : "memory"); }
template <int OFF> DI void rdtr(s16x4& d, unsigned a) { asm volatile("ds_read_b64_tr_b16 %0, %1 offset:%2" : "=v"(d) : "v"(a), "i"(OFF) : "memory"); }
typedef __bf16 bf16x2_t __attribute__((ext_vector_type(2)));
DI void gload4(u32x4& d, const void* p) { asm volatile("global_load_dwordx4 %0, %1, off" : "=v"(d) : "v"(p) : "memory"); }
DI void gload1(float& d, const void* p) { asm volatile("global_load_dword %0, %1, off" : "=v"(d) : "v"(p) : "memory"); }
DI void gload2(u32x2& d, const void* p) { asm volatile("global_load_dwordx2 %0, %1, off" : "=v"(d) : "v"(p) : "memory"); }
template <class T> DI void pin(T& v) { asm volatile("" : "+v"(v)); }
template <class T> DI void keep(const T& v) { asm volatile("" :: "v"(v)); }
DI rsrc_t mk_rsrc(const void* p, unsigned n = 0x7fffffff) { return __builtin_amdgcn_make_buffer_rsrc((void*)p, 0, n, 0x00020000); }
DI void dma16(rsrc_t r, unsigned voff, unsigned soff, LAS char* l) { __builtin_amdgcn_raw_ptr_buffer_load_lds(r, (LAS void*)l, 16, voff, soff, 0, 0); }
DI unsigned cvt_pk(float lo, float hi) { unsigned r; asm("v_cvt_pk_bf16_f32 %0, %1, %2" : "=v"(r) : "v"(lo), "v"(hi)); return r; }
DI float bflo(unsigned w) { return __uint_as_float(w << 16); }
DI float bfhi(unsigned w) { return __uint_as_float(w & 0xffff0000u); }
constexpr float LOG2E = 1.44269504088896f;
DI float phi_f(float t) { const float e = __builtin_amdgcn_exp2f(__builtin_fminf(t * LOG2E, 0.f)); return e + __builtin_fmaxf(t + 1e-6f, 1e-6f); }
DI f32x2 mish2(f32x2 h) {
  const f32x2 t = h * LOG2E;
  f32x2 e; e.x = __builtin_amdgcn_exp2f(t.x); e.y = __builtin_amdgcn_exp2f(t.y);
  const f32x2 a = __builtin_elementwise_fma(e, (f32x2){0.5f, 0.5f}, (f32x2){1.f, 1.f});
  const f32x2 d = __builtin_elementwise_fma(e, a, (f32x2){1.f, 1.f});
  f32x2 r; r.x = __builtin_amdgcn_rcpf(d.x); r.y = __builtin_amdgcn_rcpf(d.y);
  return __builtin_elementwise_fma(-h, r, h);
}
DI f32x4 mish4(const f32x4& v) { const f32x2 a = mish2((f32x2){v[0], v[1]}), b = mish2((f32x2){v[2], v[3]}); return (f32x4){a.x, a.y, b.x, b.y}; }
DI f32x4 phi4(const f32x4& v) { return (f32x4){phi_f(v[0]), phi_f(v[1]), phi_f(v[2]), phi_f(v[3])}; }
#define MFMA16(a, b, c) __builtin_amdgcn_mfma_f32_16x16x32_bf16((a), (b), (c), 0, 0, 0)
#define CAT(L, H) __builtin_shufflevector((L), (H), 0, 1, 2, 3, 4, 5, 6, 7)
template <class F, int... I> DI void static_for_impl(F&& f, std::integer_sequence<int, I...>) { (f(std::integral_constant<int, I>{}), ...); }
template <int N, class F> DI void static_for(F&& f) { static_for_impl(f, std::make_integer_sequence<int, N>{}); }

constexpr int K1_XS = 32768, K1_WS = 26624, K1_WOFF = 3 * K1_XS;
constexpr int K2_XS = 16384, K2_WOFF = 98304, K2_SCR = 65536, LDS_BYTES = 163840;

struct K1Args {
  const u16* x; const u16* wqkv; const u16* w1; const u16* b1; const u16* gw; char* ws; const u16* w2; const u16* wo; u16* out;
};
typedef unsigned long long u64;
typedef __attribute__((address_space(1))) u64 gu64;
#define RLX_AGENT __ATOMIC_RELAXED, __HIP_MEMORY_SCOPE_AGENT
constexpr int SC1 = 16;
DI void touch(unsigned& dummy, const void* p) { asm volatile("global_load_dword %0, %1, off" : "+v"(dummy) : "v"(p) : "memory"); }
DI void poll_issue(u64& d, const void* p) { asm volatile("global_load_dwordx2 %0, %1, off sc1" : "=v"(d) : "v"(p) : "memory"); }
DI void wait_all(const void* p, u64 tag) {
  for (unsigned spins = 0; spins < (1u << 20); ++spins) {
    const u64 v = __hip_atomic_load((const gu64*)p, RLX_AGENT);
    if (__all(v == tag)) return;
    __builtin_amdgcn_s_sleep(1);
  }
}

template <bool KV, int WN> constexpr int blk_panel(int b) { return b == 6 ? 3 : (KV ? (b >> 1) : (b < 4 ? WN : 2)); }
template <bool KV, int WN> constexpr int blk_cb(int b) { return b == 6 ? 0 : (KV ? 2 * WN + (b & 1) : (b < 4 ? b : 2 * WN + (b & 1))); }
template <bool KV, int WN> constexpr bool blk_std(int b) { return KV && b < 4; }
template <bool KV, int WN> constexpr bool blk_bias(int b) { return KV ? b >= 4 : (WN == 1 || b >= 4); }

template <bool KV, int WN> DI void k1_epilogue(const K1Args& A, LAS char* lds, f32x4 (&acc)[4][7], int panel, int hp, int wm);
template <bool KV, int WN>
DI void k1_body(const K1Args& A, LAS char* lds, int panel, int hp, int j32, int wm, int lane) {
  constexpr int NB = 7;
  const int wid = WN * 4 + wm;
#define STAMP_AT(i)
  const int G = hp >> 1, a = hp & 1;
  const int g4 = lane >> 4, l15 = lane & 15;
  const int c0 = (KV ? 1024 : 0) + 64 * hp;
  const int c1 = KV ? (2048 + 64 * hp) : (448 * G + 128 + 128 * a);
  const int c2 = KV ? (448 * G + 64 * a) : (448 * G + 128 + 128 * a + 64);
  const int c3 = 448 * G + 384 + (KV ? 0 : 32) + 16 * a;
  auto pcol = [&](int P) { return P == 0 ? c0 : P == 1 ? c1 : P == 2 ? c2 : c3; };
  f32x4 acc[4][NB];
  static_for<NB>([&](auto B) {
    constexpr int b = B;
    f32x4 init = {0.f, 0.f, 0.f, 0.f};
    if constexpr (blk_bias<KV, WN>(b)) {
      const u32x2 w = *(const u32x2*)((const char*)A.b1 + (pcol(blk_panel<KV, WN>(b)) + 16 * blk_cb<KV, WN>(b) + 4 * g4) * 2);
      init = (f32x4){bflo(w.x), bfhi(w.x), bflo(w.y), bfhi(w.y)};
    }
#pragma unroll
    for (int mb = 0; mb < 4; ++mb) acc[mb][b] = init;
  });
  const unsigned ldsb = (unsigned)(size_t)lds;
  unsigned xcur = ldsb + (64 * wm + l15) * 128 + ((g4 ^ ((lane >> 1) & 7)) * 16);
  const int q4 = l15 >> 2, p4 = lane & 3;
  const int sr = ((q4 >> 1) & 1) | ((g4 & 1) << 1);
  unsigned aw[4];
#pragma unroll
  for (int c = 0; c < 4; ++c) aw[c] = ldsb + K1_WOFF + (8 * g4 + q4) * 128 + ((c ^ sr) * 32) + p4 * 8;
  unsigned an[2];
#pragma unroll
  for (int t = 0; t < 2; ++t) an[t] = ldsb + K1_WOFF + 24576 + (8 * g4 + ((4 * t) ^ (4 * (g4 & 1))) + q4) * 32 + p4 * 8;

  bf16x8 Af[4]; s16x4 Bl[NB], Bh[NB];
  auto readA = [&](auto MB, unsigned addr) { rd128<MB * 2048>(Af[MB], addr); };
  auto readBh = [&](auto WP, auto KS, auto B, auto H) {
    constexpr int b = B, P = blk_panel<KV, WN>(b), c = blk_cb<KV, WN>(b), wo = WP * K1_WS;
    if constexpr (P < 3) { if constexpr (H == 0) rdtr<wo + P * 8192 + KS * 4096>(Bl[b], aw[c]); else rdtr<wo + P * 8192 + KS * 4096 + 512>(Bh[b], aw[c]); }
    else { if constexpr (H == 0) rdtr<wo + KS * 1024>(Bl[b], an[0]); else rdtr<wo + KS * 1024>(Bh[b], an[1]); }
  };
  auto mma = [&](auto B, auto MB) {
    constexpr int b = B, mb = MB;
    if constexpr (blk_std<KV, WN>(b)) acc[mb][b] = MFMA16(Af[mb], CAT(Bl[b], Bh[b]), acc[mb][b]);
    else acc[mb][b] = MFMA16(CAT(Bl[b], Bh[b]), Af[mb], acc[mb][b]);
  };
  using I0 = std::integral_constant<int, 0>; using I1 = std::integral_constant<int, 1>;
  using I2 = std::integral_constant<int, 2>; using I3 = std::integral_constant<int, 3>;
  using I5 = std::integral_constant<int, 5>; using I6 = std::integral_constant<int, 6>;
  auto h1_reads = [&](auto U, auto WP, auto KS, unsigned xa_) {
    constexpr int u = U;
    if constexpr (WN == 0) {
      if constexpr (u == 0) { readA(I0{}, xa_); readBh(WP, KS, I6{}, I0{}); }
      else if constexpr (u == 1) { readA(I1{}, xa_); readBh(WP, KS, I6{}, I1{}); }
      else readBh(WP, KS, std::integral_constant<int, ((u - 2) >> 1)>{}, std::integral_constant<int, (u & 1)>{});
    } else {
      if constexpr (u == 0) readA(I0{}, xa_);
      else if constexpr (u == 1) readA(I1{}, xa_);
      else if constexpr (u < 4) readBh(WP, KS, I6{}, std::integral_constant<int, (u & 1)>{});
      else readBh(WP, KS, std::integral_constant<int, ((u - 4) >> 1)>{}, std::integral_constant<int, (u & 1)>{});
    }
  };
  constexpr int L0 = WN == 0 ? 14 : 12, L1 = 26 - L0;
  auto step = [&](auto WP, auto KS, unsigned xcur, auto WPN, auto KSN, unsigned xnext, auto BARRIER) {
    static_for<L0>([&](auto S) {
      constexpr int s = S;
      if constexpr (s == 0 || s == 2 || s == 4) WAIT_L(8);
      if constexpr (s == 6) WAIT_L(6);
      if constexpr (s == 8) WAIT_L(4);
      if constexpr (s == 10) WAIT_L(2);
      if constexpr ((s & 1) == 0) SCHED();
      if constexpr (s < 12) mma(std::integral_constant<int, (s >> 1)>{}, std::integral_constant<int, (s & 1)>{}); else mma(I6{}, std::integral_constant<int, (s & 1)>{});
      if constexpr (s == 0) readBh(WP, KS, I5{}, I0{});
      if constexpr (s == 1) readBh(WP, KS, I5{}, I1{});
      if constexpr (s == 2) readA(I2{}, xcur);
      if constexpr (s == 3) readA(I3{}, xcur);
      SCHED();
    });
    WAIT_L(0);
    if constexpr (decltype(BARRIER)::value) {
      BAR();
      if constexpr (WN == 0) __builtin_amdgcn_s_setprio(2);
    }
    else { if constexpr (WN == 0) __builtin_amdgcn_s_setprio(0); }
    SCHED();
    static_for<L1>([&](auto U) {
      constexpr int u = U;
      if constexpr (WN == 0) mma(std::integral_constant<int, (u >> 1)>{}, std::integral_constant<int, 2 + (u & 1)>{});
      else { if constexpr (u < 2) mma(I6{}, std::integral_constant<int, 2 + u>{}); else mma(std::integral_constant<int, ((u - 2) >> 1)>{}, std::integral_constant<int, 2 + (u & 1)>{}); }
      h1_reads(U, WPN, KSN, xnext);
      SCHED();
    });
  };
  STAMP_AT(1);
  BAR(); SCHED();
  STAMP_AT(2);
  static_for<L1>([&](auto U) { h1_reads(U, I0{}, I0{}, xcur); });
  if constexpr (WN == 0) __builtin_amdgcn_s_setprio(2); else __builtin_amdgcn_s_setprio(1);
  int xs = 0;
  auto ktile = [&](auto WP, int t) {
    unsigned x1 = xcur ^ 64u, xn = xcur + (xs == 2 ? (unsigned)(-2 * K1_XS) : (unsigned)K1_XS);
    pin(x1); pin(xn);
    step(WP, I0{}, xcur, WP, I1{}, x1, std::false_type{});
    step(WP, I1{}, x1, std::integral_constant<int, WP ^ 1>{}, I0{}, xn, std::true_type{});
    xcur = xn; xs = xs == 2 ? 0 : xs + 1;
  };
#pragma unroll 1
  for (int t = 0; t < 16; t += 2) { ktile(I0{}, t); ktile(I1{}, t + 1); }
  SCHED(); WAIT_L(0); SCHED();
  __builtin_amdgcn_s_setprio(0);
  STAMP_AT(3);

  k1_epilogue<KV, WN>(A, lds, acc, panel, hp, wm);
  WAIT_V(0);
}

template <bool KV, int WN>
DI void k1_epilogue(const K1Args& A, LAS char* lds, f32x4 (&acc)[4][7], int panel, int hp, int wm) {
  int lane = threadIdx.x & 63; asm volatile("" : "+v"(lane));
  const int g4 = lane >> 4, l15 = lane & 15;
  const int G = hp >> 1, a = hp & 1;
  const int c1 = 448 * G + 128 + 128 * a;
  const int c2 = KV ? (448 * G + 64 * a) : (448 * G + 128 + 128 * a + 64);
  const rsrc_t rws = mk_rsrc(A.ws);
  const unsigned rowbase = (unsigned)panel * 256 + 64 * wm;
  const int chunk_g = (g4 & 1) * 2 + (g4 >> 1);
  auto pack2 = [&](const f32x4& e, const f32x4& o) {
    const unsigned e0 = cvt_pk(e[0], e[1]), e1 = cvt_pk(e[2], e[3]), o0 = cvt_pk(o[0], o[1]), o1 = cvt_pk(o[2], o[3]);
    const auto r0 = __builtin_amdgcn_permlane16_swap(e0, o0, false, false);
    const auto r1 = __builtin_amdgcn_permlane16_swap(e1, o1, false, false);
    return (u32x4){r0[0], r1[0], r0[1], r1[1]};
  };
  auto flush_rows = [&](LAS char* tile, int i0, int i1, int col0) {
#pragma unroll
    for (int i = i0; i < i1; ++i) {
      const u32x4 w = *(LAS u32x4*)(tile + i * 1024 + lane * 16);
      const int r = 8 * i + (lane >> 3), ch = (lane & 7) ^ (r & 7);
      __builtin_amdgcn_raw_buffer_store_b128(w, rws, (unsigned)(WS_XA + ((rowbase + r) * XA_LD + col0 + ch * 8) * 2), 0, SC1);
    }
  };
  LAS char* t2 = lds + wm * 8192;
  if constexpr (!KV && WN == 0) {
    LAS char* tq = lds + K2_SCR + wm * 8192;
#pragma unroll
    for (int mb = 0; mb < 4; ++mb)
#pragma unroll
      for (int p = 0; p < 2; ++p) *(LAS u32x4*)(tq + (16 * mb + l15) * 128 + (((4 * p + chunk_g) ^ (l15 & 7)) * 16)) = pack2(acc[mb][2 * p], acc[mb][2 * p + 1]);
  }
  LAS char* sc = lds + 2 * K1_XS;
  LAS char* sz = lds + K1_XS;
  if constexpr (KV) {
    f32x4 kvD[4]; f32x4 zD[2];
#pragma unroll
    for (int i = 0; i < 4; ++i) kvD[i] = (f32x4){0.f, 0.f, 0.f, 0.f};
    zD[0] = zD[1] = (f32x4){0.f, 0.f, 0.f, 0.f};
    const bf16x8 ones = {0x3f80, 0x3f80, 0x3f80, 0x3f80, 0x3f80, 0x3f80, 0x3f80, 0x3f80};
#pragma unroll
    for (int mp = 0; mp < 2; ++mp) {
      bf16x8 ka[2], vb[2];
#pragma unroll
      for (int b = 0; b < 2; ++b) {
        const f32x4 k0 = phi4(acc[2 * mp][b]), k1 = phi4(acc[2 * mp + 1][b]);
        const u32x4 w = {cvt_pk(k0[0], k0[1]), cvt_pk(k0[2], k0[3]), cvt_pk(k1[0], k1[1]), cvt_pk(k1[2], k1[3])};
        ka[b] = __builtin_bit_cast(bf16x8, w);
        const f32x4 v0 = acc[2 * mp][2 + b], v1 = acc[2 * mp + 1][2 + b];
        const u32x4 u = {cvt_pk(v0[0], v0[1]), cvt_pk(v0[2], v0[3]), cvt_pk(v1[0], v1[1]), cvt_pk(v1[2], v1[3])};
        vb[b] = __builtin_bit_cast(bf16x8, u);
      }
#pragma unroll
      for (int db = 0; db < 2; ++db) {
#pragma unroll
        for (int eb = 0; eb < 2; ++eb) kvD[db * 2 + eb] = MFMA16(ka[db], vb[eb], kvD[db * 2 + eb]);
        zD[db] = MFMA16(ka[db], ones, zD[db]);
      }
    }
#pragma unroll
    for (int i = 0; i < 4; ++i) *(LAS f32x4*)(sc + ((WN * 4 + wm) * 4 + i) * 1024 + lane * 16) = kvD[i];
    if (l15 == 0) {
#pragma unroll
      for (int db = 0; db < 2; ++db) *(LAS f32x4*)(sz + ((WN * 4 + wm) * 2 + db) * 64 + g4 * 16) = zD[db];
    }
  }
#pragma unroll
  for (int mb = 0; mb < 4; ++mb) *(LAS u32x4*)(t2 + (16 * mb + l15) * 128 + (((4 * WN + chunk_g) ^ (l15 & 7)) * 16)) = pack2(mish4(acc[mb][4]), mish4(acc[mb][5]));
#pragma unroll
  for (int m2 = 0; m2 < 2; ++m2) {
    const f32x4 v = mish4(acc[2 * WN + m2][6]);
    const u32x2 w = {cvt_pk(v[0], v[1]), cvt_pk(v[2], v[3])};
    __builtin_amdgcn_raw_buffer_store_b64(w, rws, (unsigned)(WS_NB + ((G * 4 + (KV ? 0 : 2) + a) * 2048 + rowbase + 16 * (2 * WN + m2) + l15) * 32 + g4 * 8), 0, SC1);
  }
  WAIT_L(0); BAR(); SCHED();
  flush_rows(t2, 4 * WN, 4 * WN + 4, c2);
  if constexpr (KV) {
    const int i4 = wm * 64 + lane, e = i4 >> 3, d0 = (i4 & 7) * 4;
    const int off = (((d0 >> 4) * 2 + (e >> 4)) * 64 + (e & 15) + 16 * ((d0 & 15) >> 2)) * 16;
    f32x4 s = {0.f, 0.f, 0.f, 0.f};
#pragma unroll
    for (int m = 0; m < 4; ++m) s += *(LAS f32x4*)(sc + (WN * 4 + m) * 4096 + off);
    __builtin_amdgcn_raw_buffer_store_b128(__builtin_bit_cast(u32x4, s), rws, (unsigned)(WS_KV + ((panel * 32 + 2 * hp + WN) * 1024 + i4 * 4) * 4), 0, SC1);
    if (wm == 0 && lane < 8) {
      const int db = lane >> 2, gq = lane & 3;
      f32x4 zs = {0.f, 0.f, 0.f, 0.f};
#pragma unroll
      for (int m = 0; m < 4; ++m) zs += *(LAS f32x4*)(sz + ((WN * 4 + m) * 2 + db) * 64 + gq * 16);
      __builtin_amdgcn_raw_buffer_store_b128(__builtin_bit_cast(u32x4, zs), rws, (unsigned)(WS_Z + ((panel * 32 + 2 * hp + WN) * 32 + 16 * db + 4 * gq) * 4), 0, SC1);
    }
  } else if constexpr (WN == 1) {
    LAS char* t1 = lds + K1_XS + wm * 8192;
#pragma unroll
    for (int mb = 0; mb < 4; ++mb) {
#pragma unroll
      for (int p = 0; p < 2; ++p) *(LAS u32x4*)(t1 + (16 * mb + l15) * 128 + (((4 * p + chunk_g) ^ (l15 & 7)) * 16)) = pack2(mish4(acc[mb][2 * p]), mish4(acc[mb][2 * p + 1]));
      flush_rows(t1, 2 * mb, 2 * mb + 2, c1);
    }
  }
}

DI void k2_compute(const K1Args& A, LAS char* lds, int tm, int tn, int role, int panel, bool isq, int hp, int wid) {
  int lane = threadIdx.x & 63; asm volatile("" : "+v"(lane));
  const int nt = role == 0 ? K2_NA + 16 : 56 - K2_NA, tile = tm * 8 + tn;
  const int wm = wid >> 2, wn = wid & 3;
  const int g4 = lane >> 4, l15 = lane & 15;
  BAR();
  bf16x8 pf[2][2];
  if (isq) {
#pragma unroll
    for (int mb = 0; mb < 2; ++mb)
#pragma unroll
      for (int h = 0; h < 2; ++h) {
        const int r = 32 * wid + 16 * mb + l15;
        const u32x4 raw = *(LAS u32x4*)(lds + K2_SCR + r * 128 + (((4 * h + g4) ^ (r & 7)) * 16));
        u32x4 w;
#pragma unroll
        for (int i = 0; i < 4; ++i) w[i] = cvt_pk(phi_f(bflo(raw[i])), phi_f(bfhi(raw[i])));
        pf[mb][h] = __builtin_bit_cast(bf16x8, w);
      }
  }
  BAR();
  const unsigned prow = (unsigned)panel * 256 + 32 * wid;
  const rsrc_t rws = mk_rsrc(A.ws);
  u32x4 part[8], zq; unsigned gv[2], gq[4];
  if (isq) {
#pragma unroll
    for (int q = 0; q < 8; ++q) part[q] = __builtin_amdgcn_raw_buffer_load_b128(rws, (unsigned)(WS_KV + ((q * 32 + 2 * hp) * 1024 + (wid * 64 + lane) * 4) * 4), 0, SC1);
    zq = __builtin_amdgcn_raw_buffer_load_b128(rws, (unsigned)(WS_Z + ((lane >> 3) & 7) * 4096 + (2 * hp * 32 + (wid & 1) * 32 + (lane & 7) * 4) * 4), 0, SC1);
#pragma unroll
    for (int mb = 0; mb < 2; ++mb) {
      const int r = 32 * wid + 16 * mb + l15;
      (void)r;
      gv[mb] = __builtin_amdgcn_raw_buffer_load_b32(rws, (unsigned)(WS_G + (prow + 16 * mb + l15) * 4), 0, SC1);
    }
  }
#pragma unroll
  for (int mb = 0; mb < 4; ++mb) gq[mb] = __builtin_amdgcn_raw_buffer_load_b32(rws, (unsigned)(WS_G + (tm * 128 + 64 * wm + 16 * mb + l15) * 4), 0, SC1);
  const unsigned ldsb = (unsigned)(size_t)lds;
  unsigned ax[2];
#pragma unroll
  for (int ks = 0; ks < 2; ++ks) ax[ks] = ldsb + (64 * wm + l15) * 128 + (((4 * ks + g4) ^ ((lane >> 1) & 7)) * 16);
  const int q4 = l15 >> 2, p4 = lane & 3;
  const int sr = ((q4 >> 1) & 1) | ((g4 & 1) << 1);
  unsigned aw[2];
#pragma unroll
  for (int nb = 0; nb < 2; ++nb) aw[nb] = ldsb + K2_WOFF + (wn >> 1) * 8192 + (8 * g4 + q4) * 128 + (((2 * (wn & 1) + nb) ^ sr) * 32) + p4 * 8;
  f32x4 acc[4][2];
#pragma unroll
  for (int i = 0; i < 4; ++i) acc[i][0] = acc[i][1] = (f32x4){0.f, 0.f, 0.f, 0.f};
  bf16x8 Af[2][2][4]; s16x4 Bl[2][2][2], Bh[2][2][2];
  auto rd = [&](auto BUF, auto I, const unsigned (&pa)[2], const unsigned (&pw)[2]) {
    constexpr int i = I, ks = i >> 3, r = i & 7;
    if constexpr (r < 2) { if constexpr (r == 0) rdtr<ks * 4096>(Bl[BUF][ks][0], pw[0]); else rdtr<ks * 4096 + 512>(Bh[BUF][ks][0], pw[0]); }
    else if constexpr (r < 6) rd128<(r - 2) * 2048>(Af[BUF][ks][r - 2], pa[ks]);
    else { if constexpr (r == 6) rdtr<ks * 4096>(Bl[BUF][ks][1], pw[1]); else rdtr<ks * 4096 + 512>(Bh[BUF][ks][1], pw[1]); }
  };
  using I0 = std::integral_constant<int, 0>; using I1 = std::integral_constant<int, 1>;
  LAS char* kvs = lds + K2_SCR;
  f32x4 ksum = {0.f, 0.f, 0.f, 0.f}, zsum = {0.f, 0.f, 0.f, 0.f};
  if (isq) {
#pragma unroll
    for (int q = 0; q < 8; ++q) ksum += __builtin_bit_cast(f32x4, part[q]);
    zsum = __builtin_bit_cast(f32x4, zq);
    if (wid < 2) {
#pragma unroll
      for (int o = 8; o <= 32; o <<= 1) { zsum[0] += __shfl_xor(zsum[0], o); zsum[1] += __shfl_xor(zsum[1], o); zsum[2] += __shfl_xor(zsum[2], o); zsum[3] += __shfl_xor(zsum[3], o); }
    }
    pin(pf[0][0]); pin(pf[0][1]); pin(pf[1][0]); pin(pf[1][1]);
  }
  WAIT_L(0);
  STAMP_AT(1);
  BAR(); SCHED();
  STAMP_AT(2);
  if (isq) {
    *(LAS u32x2*)(kvs + (wid * 64 + lane) * 8) = (u32x2){cvt_pk(ksum[0], ksum[1]), cvt_pk(ksum[2], ksum[3])};
    if (wid < 2 && lane < 8) *(LAS u32x2*)(kvs + 4096 + wid * 64 + lane * 8) = (u32x2){cvt_pk(zsum[0], zsum[1]), cvt_pk(zsum[2], zsum[3])};
  }
  SCHED();
  static_for<16>([&](auto I) { rd(I0{}, I, ax, aw); });
  auto body = [&](auto CUR, int i) {
    constexpr int cur = CUR;
    const unsigned so1 = ((i + 1) & 3) * K2_XS;
    unsigned pa[2] = {ax[0] + so1, ax[1] + so1}, pw[2] = {aw[0] + so1, aw[1] + so1};
    pin(pa[0]); pin(pa[1]); pin(pw[0]); pin(pw[1]);
    WAIT_L(0); BAR(); SCHED();
    static_for<16>([&](auto S) {
      constexpr int s = S, ks = s >> 3, nb = (s >> 2) & 1, mb = s & 3;
      acc[mb][nb] = MFMA16(CAT(Bl[cur][ks][nb], Bh[cur][ks][nb]), Af[cur][ks][mb], acc[mb][nb]);
      if constexpr (s < 8) { rd(std::integral_constant<int, cur ^ 1>{}, S, pa, pw); }
      else if constexpr (s < 12) { rd(std::integral_constant<int, cur ^ 1>{}, std::integral_constant<int, 2 * s - 8>{}, pa, pw); rd(std::integral_constant<int, cur ^ 1>{}, std::integral_constant<int, 2 * s - 7>{}, pa, pw); }
      SCHED();
    });
  };
  body(I0{}, 0);
  if (isq) {
    const unsigned kb = (unsigned)(size_t)kvs;
    const int chunk_g = (g4 & 1) * 2 + (g4 >> 1);
#pragma unroll
    for (int h = 0; h < 2; ++h) {
      bf16x8 ka[2], za;
      rd128<0>(ka[0], kb + h * 2048 + l15 * 64 + g4 * 16); rd128<1024>(ka[1], kb + h * 2048 + l15 * 64 + g4 * 16); rd128<4096>(za, kb + h * 64 + g4 * 16);
      WAIT_L(0); SCHED();
#pragma unroll
      for (int mb = 0; mb < 2; ++mb) {
        const f32x4 zero = {0.f, 0.f, 0.f, 0.f};
        const f32x4 n0 = MFMA16(ka[0], pf[mb][h], zero), n1 = MFMA16(ka[1], pf[mb][h], zero), dn = MFMA16(za, pf[mb][h], zero);
        const float sc = __uint_as_float(gv[mb]) * __builtin_amdgcn_rcpf(dn[0] + 1e-6f);
        const f32x4 e = n0 * sc, o = n1 * sc;
        const unsigned e0 = cvt_pk(e[0], e[1]), e1 = cvt_pk(e[2], e[3]), o0 = cvt_pk(o[0], o[1]), o1 = cvt_pk(o[2], o[3]);
        const auto r0 = __builtin_amdgcn_permlane16_swap(e0, o0, false, false);
        const auto r1 = __builtin_amdgcn_permlane16_swap(e1, o1, false, false);
        const u32x4 w = {r0[0], r1[0], r0[1], r1[1]};
        __builtin_amdgcn_raw_buffer_store_b128(w, rws, (unsigned)(WS_XA + ((prow + 16 * mb + l15) * XA_LD + 3584 + 64 * hp + 32 * h + chunk_g * 8) * 2), 0, SC1);
      }
    }
    SCHED();
  }
  body(I1{}, 1);
  body(I0{}, 2);
  float omg[4];
#pragma unroll
  for (int mb = 0; mb < 4; ++mb) omg[mb] = 1.f - __uint_as_float(gq[mb]);
  body(I1{}, 3);
  WAIT_V(0);
  WAIT_L(0); SCHED();
  const int n1 = role == 0 ? K2_NA : nt;
#pragma unroll 1
  for (int i = 4; i < n1; i += 2) { body(I0{}, i); body(I1{}, i + 1); }
  SCHED(); WAIT_L(0); SCHED();
#pragma unroll
  for (int mb = 0; mb < 4; ++mb) { acc[mb][0] *= omg[mb]; acc[mb][1] *= omg[mb]; }
#pragma unroll 1
  for (int i = n1; i < nt; i += 2) { body(I0{}, i); body(I1{}, i + 1); }
  SCHED(); WAIT_L(0); SCHED();
  STAMP_AT(3);
  if (role == 1) {
    const unsigned poff = (unsigned)(WS_PART + (tile * 8 + wid) * 4096 + lane * 16);
#pragma unroll
    for (int mb = 0; mb < 4; ++mb) {
      const u32x4 w = {cvt_pk(acc[mb][0][0], acc[mb][0][1]), cvt_pk(acc[mb][0][2], acc[mb][0][3]), cvt_pk(acc[mb][1][0], acc[mb][1][1]), cvt_pk(acc[mb][1][2], acc[mb][1][3])};
      __builtin_amdgcn_raw_buffer_store_b128(w, rws, poff + mb * 1024, 0, SC1);
    }
    STAMP_AT(4);
    WAIT_V(0);
    BAR();
  } else {
    BAR();
    BAR();
    STAMP_AT(4);
    const int chunk_g = (g4 & 1) * 2 + (g4 >> 1);
    LAS char* pl = lds + K2_SCR + wid * 4096 + lane * 16;
#pragma unroll
    for (int mb = 0; mb < 4; ++mb) {
      const u32x4 pw_ = *(LAS u32x4*)(pl + mb * 1024);
      const f32x4 e = acc[mb][0] + (f32x4){bflo(pw_[0]), bfhi(pw_[0]), bflo(pw_[1]), bfhi(pw_[1])}, o = acc[mb][1] + (f32x4){bflo(pw_[2]), bfhi(pw_[2]), bflo(pw_[3]), bfhi(pw_[3])};
      const unsigned e0 = cvt_pk(e[0], e[1]), e1 = cvt_pk(e[2], e[3]), o0 = cvt_pk(o[0], o[1]), o1 = cvt_pk(o[2], o[3]);
      const auto r0 = __builtin_amdgcn_permlane16_swap(e0, o0, false, false);
      const auto r1 = __builtin_amdgcn_permlane16_swap(e1, o1, false, false);
      const u32x4 w = {r0[0], r1[0], r0[1], r1[1]};
      *(u32x4*)(A.out + (size_t)(tm * 128 + 64 * wm + 16 * mb + l15) * 1024 + tn * 128 + 32 * wn + chunk_g * 8) = w;
    }
  }
}

DI void loader(const K1Args& A, LAS char* lds, bool isx, int lane) {
  const int b = blockIdx.x, xcd = b & 7, idx = b >> 3, rq = xcd >> 1, ch = xcd & 1;
  const int panel = 2 * rq + (idx >> 4), c16 = idx & 15, hp = 8 * ch + (c16 >> 1), j32 = ch * 16 + c16;
  const bool kv = (c16 & 1) != 0;
  u64 ep; poll_issue(ep, A.ws + WS_EP);
  const int G = hp >> 1, a = hp & 1;
  const int c0 = (kv ? 1024 : 0) + 64 * hp;
  const int c1 = kv ? (2048 + 64 * hp) : (448 * G + 128 + 128 * a);
  const int c2 = kv ? (448 * G + 64 * a) : (448 * G + 128 + 128 * a + 64);
  const int c3 = 448 * G + 384 + (kv ? 0 : 32) + 16 * a;
  unsigned chx[2], chw[2];
#pragma unroll
  for (int par = 0; par < 2; ++par) {
    chx[par] = (unsigned)(((lane & 7) ^ ((4 * par + (lane >> 4)) & 7)) * 16);
    chw[par] = (unsigned)(((lane & 7) ^ ((((lane >> 4) & 1) | (par << 1)) << 1)) * 16);
  }
  unsigned tdummy = 0;
  u64 tag;
  {
    unsigned vA[4], vB[4], vN[2];
#pragma unroll
    for (int k = 0; k < 4; ++k) {
      vA[k] = (isx ? (unsigned)((8 * k + (lane >> 3)) * 2048) + chx[k & 1] : (unsigned)((8 * k + (lane >> 3)) * 6144) + chw[k & 1]) + 3072 - k * 1024;
      vB[k] = (unsigned)((8 * k + (lane >> 3)) * 7168) + chw[k & 1] + 3072 - k * 1024;
    }
#pragma unroll
    for (int n = 0; n < 2; ++n) { const int rho = 32 * n + (lane >> 1); vN[n] = (unsigned)((rho ^ (((rho >> 3) & 1) << 2)) * 7168 + (lane & 1) * 16) + 3072 - n * 1024; }
    const rsrc_t rx = mk_rsrc((const char*)A.x - 3072), rqk = mk_rsrc((const char*)A.wqkv - 3072), r1 = mk_rsrc((const char*)A.w1 - 3072);
    const unsigned xrow0 = (unsigned)panel * 256 * 2048;
    auto issueX = [&](int t, int xs) {
#pragma unroll
      for (int g = 0; g < 8; ++g)
        static_for<4>([&](auto K) { __builtin_amdgcn_raw_ptr_buffer_load_lds(rx, (LAS void*)(lds + xs * K1_XS + g * 4096), 16, vA[K], xrow0 + t * 128 + g * 65536, K * 1024, 0); });
    };
    auto issueW = [&](int t) {
      LAS char* dst = lds + K1_WOFF + (t & 1) * K1_WS;
#pragma unroll
      for (int g = 0; g < 2; ++g)
        static_for<4>([&](auto K) { __builtin_amdgcn_raw_ptr_buffer_load_lds(rqk, (LAS void*)(dst + g * 4096), 16, vA[K], (t * 64 + g * 32) * 6144 + c0 * 2, K * 1024, 0); });
      if (kv) {
#pragma unroll
        for (int g = 0; g < 2; ++g)
          static_for<4>([&](auto K) { __builtin_amdgcn_raw_ptr_buffer_load_lds(rqk, (LAS void*)(dst + 8192 + g * 4096), 16, vA[K], (t * 64 + g * 32) * 6144 + c1 * 2, K * 1024, 0); });
      } else {
#pragma unroll
        for (int g = 0; g < 2; ++g)
          static_for<4>([&](auto K) { __builtin_amdgcn_raw_ptr_buffer_load_lds(r1, (LAS void*)(dst + 8192 + g * 4096), 16, vB[K], (t * 64 + g * 32) * 7168 + c1 * 2, K * 1024, 0); });
      }
#pragma unroll
      for (int g = 0; g < 2; ++g)
        static_for<4>([&](auto K) { __builtin_amdgcn_raw_ptr_buffer_load_lds(r1, (LAS void*)(dst + 16384 + g * 4096), 16, vB[K], (t * 64 + g * 32) * 7168 + c2 * 2, K * 1024, 0); });
      static_for<2>([&](auto N) { __builtin_amdgcn_raw_ptr_buffer_load_lds(r1, (LAS void*)(dst + 24576), 16, vN[N], t * 64 * 7168 + c3 * 2, N * 1024, 0); });
    };
    auto touch3 = [&]() {
      const int w = b * 2 + (isx ? 0 : 1);
#pragma unroll
      for (int n = 0; n < 3; ++n) {
        const int k = n * 64 + lane, li = w * 144 + (k < 144 ? k : 143);
        touch(tdummy, li < 57344 ? (const char*)A.w2 + (size_t)li * 128 : (const char*)A.wo + (size_t)(li - 57344) * 128);
      }
    };
    if (isx) {
      issueX(0, 0); issueX(1, 1);
#pragma unroll
      for (int n = 0; n < 4; ++n) touch(tdummy, (const char*)A.x + (size_t)(panel * 256 + 16 * c16 + 4 * n + (lane >> 4)) * 2048 + (lane & 15) * 128);
      auto wtouch = [&](int t0) {
        const int li = b * 52 + (lane < 52 ? lane : 51);
        int row = 64 * t0 + li / 104; row = row < 1024 ? row : 1023;
        const int c = li % 104;
        touch(tdummy, c < 48 ? (const char*)A.wqkv + (size_t)row * 6144 + c * 128 : (const char*)A.w1 + (size_t)row * 7168 + (c - 48) * 128);
      };
      auto wtouch1 = [&](int t0) {
        const int li = b * 26 + (lane < 26 ? lane : 25);
        const int row = 64 * (t0 < 16 ? t0 : 15) + li / 104, c = li % 104;
        touch(tdummy, c < 48 ? (const char*)A.wqkv + (size_t)row * 6144 + c * 128 : (const char*)A.w1 + (size_t)row * 7168 + (c - 48) * 128);
      };
#define WD1 4
      wtouch(2);
      WAIT_V(37); pin(ep); tag = ep + 1;
      BAR();
      if (lane == 0) __hip_atomic_store((gu64*)(A.ws + WS_START) + b, tag, RLX_AGENT);
      issueX(2, 2);
#pragma unroll
      for (int t = 4; t < WD1 + 1; ++t) wtouch1(t);
      int xs = 0;
#pragma unroll 1
      for (int t = 0; t < 13; ++t) {
        WAIT_V(33); BAR(); issueX(t + 3, xs); xs = xs == 2 ? 0 : xs + 1;
        wtouch1(t + 1 + WD1);
      }
      touch3();
      WAIT_V(35); BAR();
      WAIT_V(3); BAR();
      u32x4 gx[8][2], gg[2];
#pragma unroll
      for (int r = 0; r < 8; ++r) { const char* xr = (const char*)A.x + (size_t)(panel * 256 + j32 * 8 + r) * 2048 + lane * 32; gload4(gx[r][0], xr); gload4(gx[r][1], xr + 16); }
      gload4(gg[0], (const char*)A.gw + lane * 32); gload4(gg[1], (const char*)A.gw + lane * 32 + 16);
      BAR();
      BAR();
      WAIT_V(0);
      {
        pin(gg[0]); pin(gg[1]);
        float s[8];
#pragma unroll
        for (int r = 0; r < 8; ++r) {
          pin(gx[r][0]); pin(gx[r][1]);
          s[r] = 0.f;
#pragma unroll
          for (int h = 0; h < 2; ++h)
#pragma unroll
            for (int i = 0; i < 4; ++i) { const unsigned ux = gx[r][h][i], ug = gg[h][i]; s[r] = __builtin_amdgcn_fdot2_f32_bf16(__builtin_bit_cast(bf16x2_t, ux), __builtin_bit_cast(bf16x2_t, ug), s[r], false); }
        }
#pragma unroll
        for (int o = 32; o >= 1; o >>= 1)
#pragma unroll
          for (int r = 0; r < 8; ++r) s[r] += __shfl_xor(s[r], o);
        float mine = s[0];
#pragma unroll
        for (int r = 1; r < 8; ++r) mine = lane == r ? s[r] : mine;
        const float gval = __builtin_amdgcn_rcpf(1.f + __builtin_amdgcn_exp2f(-LOG2E * mine));
        if (lane < 8) __hip_atomic_store((__attribute__((address_space(1))) unsigned*)(A.ws + WS_G) + panel * 256 + j32 * 8 + lane, __float_as_uint(gval), RLX_AGENT);
      }
    } else {
      issueW(0); issueW(1);
      WAIT_V(26); pin(ep); tag = ep + 1;
      BAR();
#pragma unroll 1
      for (int t = 0; t < 14; ++t) { WAIT_V(0); BAR(); issueW(t + 2); }
      touch3();
      WAIT_V(3); BAR();
      BAR();
    }
  }
  const int role = (idx >> 1) & 1, tm = 4 * rq + (idx >> 3), tn = 4 * ch + 2 * ((idx >> 2) & 1) + (idx & 1), tile = tm * 8 + tn;
  const int nt = role == 0 ? K2_NA + 16 : 56 - K2_NA;
  unsigned voff[4];
#pragma unroll
  for (int k = 0; k < 4; ++k)
    voff[k] = (isx ? (unsigned)((8 * k + (lane >> 3)) * (XA_LD * 2) + chx[k & 1]) : (unsigned)((8 * k + (lane >> 3)) * 2048 + chw[k & 1])) + 3072 - k * 1024;
  const unsigned gstep = isx ? 32 * XA_LD * 2 : 32 * 2048;
  auto issue = [&](rsrc_t r, unsigned so, int slot) {
    LAS char* st = lds + (isx ? 0 : K2_WOFF) + slot * K2_XS;
#pragma unroll
    for (int g = 0; g < 4; ++g) {
      const unsigned sg = isx ? so + g * gstep : so + (g & 1) * gstep + (g >> 1) * 128;
      static_for<4>([&](auto K) { __builtin_amdgcn_raw_ptr_buffer_load_lds(r, (LAS void*)(st + g * 4096), 16, voff[K], sg, K * 1024, SC1); });
    }
  };
  unsigned voffN[4];
#pragma unroll
  for (int k = 0; k < 4; ++k) { const unsigned c = chx[k & 1] >> 4; voffN[k] = (c >> 1) * 65536 + (8 * k + (lane >> 3)) * 32 + (c & 1) * 16 + 3072 - k * 1024; }
  const rsrc_t rn = mk_rsrc((const char*)(A.ws + WS_NB) - 3072);
  auto issueN = [&](int grp, int slot) {
    LAS char* st = lds + slot * K2_XS;
#pragma unroll
    for (int g = 0; g < 4; ++g)
      static_for<4>([&](auto K) { __builtin_amdgcn_raw_ptr_buffer_load_lds(rn, (LAS void*)(st + g * 4096), 16, voffN[K], (unsigned)grp * 262144 + (unsigned)(tm * 128 + g * 32) * 32, K * 1024, SC1); });
  };
  const rsrc_t r0 = mk_rsrc(isx ? (const char*)(A.ws + WS_XA) - 3072 : (const char*)A.w2 - 3072);
  const rsrc_t r1 = mk_rsrc(isx ? (const char*)(A.ws + WS_XA) - 3072 : (const char*)A.wo - 3072);
  const unsigned tstep = isx ? 128u : 64u * 2048u;
  const unsigned sbase = isx ? (unsigned)tm * 128 * (XA_LD * 2) : (unsigned)tn * 256;
  const unsigned s0 = sbase + (role == 0 ? 0 : K2_NA) * tstep;
  const unsigned s1 = isx ? sbase + 56 * tstep : sbase;
  const rsrc_t rp = mk_rsrc(A.ws + WS_PART);
  auto issue_partial = [&]() {
    LAS char* dst = lds + K2_SCR + (isx ? 0 : 16384);
#pragma unroll
    for (int g = 0; g < 4; ++g)
      static_for<4>([&](auto K) { __builtin_amdgcn_raw_ptr_buffer_load_lds(rp, (LAS void*)(dst + g * 4096), 16, lane * 16, (unsigned)tile * 32768 + (isx ? 0 : 16384) + g * 4096, K * 1024, SC1); });
  };
  auto issueA = [&](int pos) {
    const int kt = (role == 0 ? 0 : K2_NA) + pos, grp = kt / 7;
    if (isx && kt - 7 * grp == 6) issueN(grp, pos & 3); else issue(r0, s0 + pos * tstep, pos & 3);
  };
  if (!isx) { issueA(0); issueA(1); issueA(2); issueA(3);
  }
  if (!isx) BAR();
  if (isx) WAIT_V(0);
  BAR();
  if (isx) {
    if (lane == 0) __hip_atomic_store((gu64*)(A.ws + WS_DONE1) + panel * 32 + j32, tag, RLX_AGENT);
    const int slot = lane < 32 ? panel * 32 + lane : ((lane < 40 && !kv) ? (lane - 32) * 32 + (j32 | 1) : panel * 32);
    wait_all(A.ws + WS_DONE1 + (size_t)slot * 8, tag);
  }
  BAR();
  if (isx) { issueA(0); issueA(1); issueA(2); WAIT_V(32); } else WAIT_V(48);
  BAR();
  if (isx) issueA(3);
  auto run = [&](int i0, int i1, rsrc_t r, unsigned so) {
#pragma unroll 1
    for (int i = i0; i < i1; ++i, so += tstep) { WAIT_V(32); BAR(); issue(r, so, i & 3); }
  };
  auto runA = [&](int i0, int i1) {
#pragma unroll 1
    for (int i = i0; i < i1; ++i) { WAIT_V(32); BAR(); issueA(i + 4); }
  };
  u64 pv = 0;
  runA(0, 5);
  if (isx && !kv && lane == 0) __hip_atomic_store((gu64*)(A.ws + WS_ODONE) + panel * 16 + hp, tag, RLX_AGENT);
  if (role == 0) {
    runA(5, K2_NA - 8);
    const char* od = A.ws + WS_ODONE + (size_t)(panel * 16 + (lane & 15)) * 8;
    if (isx) poll_issue(pv, (const void*)od);
    runA(K2_NA - 8, K2_NA - 4);
    if (isx) { WAIT_V(32); pin(pv); if (!__all(pv == tag)) wait_all((const void*)od, tag); }
    run(K2_NA - 4, nt - 4, r1, s1);
  } else runA(5, nt - 4);
  const char* pd = A.ws + WS_PDONE + (size_t)tile * 128;
  poll_issue(pv, (const void*)pd);
  WAIT_V(33); BAR();
  WAIT_V(17); BAR();
  WAIT_V(0);
  bool early = false;
  if (role == 0) { pin(pv); early = __all(pv == tag); }
  BAR();
  if (early) issue_partial();
  BAR();
  if (role == 0) {
    if (!early) wait_all((const void*)pd, tag);
    BAR();
    if (!early) issue_partial();
    WAIT_V(0);
    BAR();
  } else {
    BAR();
    if (isx && lane == 0) __hip_atomic_store((gu64*)(A.ws + WS_PDONE + (size_t)tile * 128), tag, RLX_AGENT);
  }
  if (b == 0 && isx) {
#pragma unroll 1
    for (int k = 0; k < 4; ++k) wait_all(A.ws + WS_START + (size_t)(k * 64 + lane) * 8, tag);
    if (lane == 0) __hip_atomic_store((gu64*)(A.ws + WS_EP), tag, RLX_AGENT);
  }
  WAIT_V(0); keep(tdummy);
}

__global__ __launch_bounds__(640, 3) void fused_kernel(K1Args A) {
  extern __shared__ __attribute__((aligned(16))) char smem[];
  LAS char* lds = (LAS char*)smem;
  const int b = blockIdx.x, xcd = b & 7, idx = b >> 3, rq = xcd >> 1, ch = xcd & 1;
  const int panel = 2 * rq + (idx >> 4), c16 = idx & 15, hp = 8 * ch + (c16 >> 1), j32 = ch * 16 + c16;
  const int wid = __builtin_amdgcn_readfirstlane(threadIdx.x >> 6), lane = threadIdx.x & 63;
  if (wid >= 8) { loader(A, lds, wid == 8, lane); return; }
  if (c16 & 1) {
    if (wid < 4) k1_body<true, 0>(A, lds, panel, hp, j32, wid, lane); else k1_body<true, 1>(A, lds, panel, hp, j32, wid - 4, lane);
  } else {
    if (wid < 4) k1_body<false, 0>(A, lds, panel, hp, j32, wid, lane); else k1_body<false, 1>(A, lds, panel, hp, j32, wid - 4, lane);
  }
  k2_compute(A, lds, 4 * rq + (idx >> 3), 4 * ch + 2 * ((idx >> 2) & 1) + (idx & 1), (idx >> 1) & 1, panel, (c16 & 1) == 0, hp, wid);
}

extern "C" void kernel_launch(void* const* d_in, const int* in_sizes, int n_in, void* d_out, int out_size, void* d_ws, size_t ws_size, hipStream_t stream) {
  K1Args a; a.x = (const u16*)d_in[0]; a.wqkv = (const u16*)d_in[1]; a.w1 = (const u16*)d_in[4]; a.b1 = (const u16*)d_in[5]; a.gw = (const u16*)d_in[7]; a.ws = (char*)d_ws;
  a.w2 = (const u16*)d_in[6]; a.wo = (const u16*)d_in[2]; a.out = (u16*)d_out;
  (void)hipFuncSetAttribute((const void*)fused_kernel, hipFuncAttributeMaxDynamicSharedMemorySize, LDS_BYTES);
  fused_kernel<<<256, 640, LDS_BYTES, stream>>>(a);
}
```

```cpp
#include <hip/hip_runtime.h>
#include <stdint.h>
#include <utility>
#define ALT 3
#define OUT_AUX 16
#define K2_NA 24
#define LAS __attribute__((address_space(3)))
#define DI __device__ __forceinline__
typedef unsigned short u16;
typedef short bf16x8 __attribute__((ext_vector_type(8)));
typedef short s16x4 __attribute__((ext_vector_type(4)));
typedef float f32x4 __attribute__((ext_vector_type(4)));
typedef float f32x2 __attribute__((ext_vector_type(2)));
typedef unsigned u32x4 __attribute__((ext_vector_type(4)));
typedef unsigned u32x2 __attribute__((ext_vector_type(2)));
typedef __amdgpu_buffer_rsrc_t rsrc_t;

constexpr int XA_LD = 4608;
constexpr size_t WS_XA = 0;
constexpr size_t WS_PQ = WS_XA + (size_t)2048 * XA_LD * 2;
constexpr size_t WS_KV = WS_PQ + (size_t)2048 * 1024 * 2;
constexpr size_t WS_Z  = WS_KV + (size_t)8 * 32 * 1024 * 4;
constexpr size_t WS_G  = WS_Z + (size_t)8 * 32 * 32 * 4;
constexpr size_t WS_EP    = WS_G + 2048 * 4;
constexpr size_t WS_DONE1 = WS_EP + 256;
constexpr size_t WS_ODONE = WS_DONE1 + 2048;
constexpr size_t WS_PDONE = WS_ODONE + 2048;
constexpr size_t WS_START = WS_PDONE + 128 * 128;
constexpr size_t WS_PART  = WS_START + 4096;
constexpr size_t WS_NB    = WS_PART + 128 * 32768;

#define SCHED() __builtin_amdgcn_sched_barrier(0)
#define WAIT_V(n) asm volatile("s_waitcnt vmcnt(%0)" ::"n"(n) : "memory")
#define WAIT_L(n) asm volatile("s_waitcnt lgkmcnt(%0)" ::"n"(n) : "memory")
#define BAR() __builtin_amdgcn_s_barrier()

template <int OFF> DI void rd128(bf16x8& d, unsigned a) { asm volatile("ds_read_b128 %0, %1 offset:%2" : "=v"(d) : "v"(a), "i"(OFF) : "memory"); }
template <int OFF> DI void rdtr(s16x4& d, unsigned a) { asm volatile("ds_read_b64_tr_b16 %0, %1 offset:%2" : "=v"(d) : "v"(a), "i"(OFF) : "memory"); }
typedef __bf16 bf16x2_t __attribute__((ext_vector_type(2)));
DI void gload4(u32x4& d, const void* p) { asm volatile("global_load_dwordx4 %0, %1, off" : "=v"(d) : "v"(p) : "memory"); }
DI void gload1(float& d, const void* p) { asm volatile("global_load_dword %0, %1, off" : "=v"(d) : "v"(p) : "memory"); }
DI void gload2(u32x2& d, const void* p) { asm volatile("global_load_dwordx2 %0, %1, off" : "=v"(d) : "v"(p) : "memory"); }
template <class T> DI void pin(T& v) { asm volatile("" : "+v"(v)); }
template <class T> DI void keep(const T& v) { asm volatile("" :: "v"(v)); }
DI rsrc_t mk_rsrc(const void* p, unsigned n = 0x7fffffff) { return __builtin_amdgcn_make_buffer_rsrc((void*)p, 0, n, 0x00020000); }
DI void dma16(rsrc_t r, unsigned voff, unsigned soff, LAS char* l) { __builtin_amdgcn_raw_ptr_buffer_load_lds(r, (LAS void*)l, 16, voff, soff, 0, 0); }
typedef __bf16 bf16x2_c __attribute__((ext_vector_type(2)));
DI unsigned cvt_pk(float lo, float hi) { return __builtin_bit_cast(unsigned, __builtin_convertvector((f32x2){lo, hi}, bf16x2_c)); }
DI float bflo(unsigned w) { return __uint_as_float(w << 16); }
DI float bfhi(unsigned w) { return __uint_as_float(w & 0xffff0000u); }
constexpr float LOG2E = 1.44269504088896f;
DI float phi_f(float t) { const float e = __builtin_amdgcn_exp2f(__builtin_fminf(t * LOG2E, 0.f)); return e + __builtin_fmaxf(t + 1e-6f, 1e-6f); }
DI f32x2 mish2(f32x2 h) {
  const f32x2 t = h * LOG2E;
  f32x2 e; e.x = __builtin_amdgcn_exp2f(t.x); e.y = __builtin_amdgcn_exp2f(t.y);
  const f32x2 a = __builtin_elementwise_fma(e, (f32x2){0.5f, 0.5f}, (f32x2){1.f, 1.f});
  const f32x2 d = __builtin_elementwise_fma(e, a, (f32x2){1.f, 1.f});
  f32x2 r; r.x = __builtin_amdgcn_rcpf(d.x); r.y = __builtin_amdgcn_rcpf(d.y);
  return __builtin_elementwise_fma(-h, r, h);
}
DI f32x4 mish4(const f32x4& v) { const f32x2 a = mish2((f32x2){v[0], v[1]}), b = mish2((f32x2){v[2], v[3]}); return (f32x4){a.x, a.y, b.x, b.y}; }
DI f32x4 phi4(const f32x4& v) { return (f32x4){phi_f(v[0]), phi_f(v[1]), phi_f(v[2]), phi_f(v[3])}; }
#define MFMA16(a, b, c) __builtin_amdgcn_mfma_f32_16x16x32_bf16((a), (b), (c), 0, 0, 0)
#define CAT(L, H) __builtin_shufflevector((L), (H), 0, 1, 2, 3, 4, 5, 6, 7)
template <class F, int... I> DI void static_for_impl(F&& f, std::integer_sequence<int, I...>) { (f(std::integral_constant<int, I>{}), ...); }
template <int N, class F> DI void static_for(F&& f) { static_for_impl(f, std::make_integer_sequence<int, N>{}); }

constexpr int K1_XS = 32768, K1_WS = 26624, K1_WOFF = 3 * K1_XS;
constexpr int K2_XS = 16384, K2_WOFF = 98304, K2_SCR = 65536, LDS_BYTES = 163840;

struct K1Args {
  const u16* x; const u16* wqkv; const u16* w1; const u16* b1; const u16* gw; char* ws; const u16* w2; const u16* wo; u16* out;
};
typedef unsigned long long u64;
typedef __attribute__((address_space(1))) u64 gu64;
#define RLX_AGENT __ATOMIC_RELAXED, __HIP_MEMORY_SCOPE_AGENT
constexpr int SC1 = 16;
DI void touch(unsigned& dummy, const void* p) { asm volatile("global_load_dword %0, %1, off" : "+v"(dummy) : "v"(p) : "memory"); }
DI void poll_issue(u64& d, const void* p) { asm volatile("global_load_dwordx2 %0, %1, off sc1" : "=v"(d) : "v"(p) : "memory"); }
DI void wait_all(const void* p, u64 tag) {
  for (unsigned spins = 0; spins < (1u << 20); ++spins) {
    const u64 v = __hip_atomic_load((const gu64*)p, RLX_AGENT);
    if (__all(v == tag)) return;
    __builtin_amdgcn_s_sleep(1);
  }
}

template <bool KV, int WN> constexpr int blk_panel(int b) { return b == 6 ? 3 : (KV ? (b >> 1) : (b < 4 ? WN : 2)); }
template <bool KV, int WN> constexpr int blk_cb(int b) { return b == 6 ? 0 : (KV ? 2 * WN + (b & 1) : (b < 4 ? b : 2 * WN + (b & 1))); }
template <bool KV, int WN> constexpr bool blk_std(int b) { return KV && b < 4; }
template <bool KV, int WN> constexpr bool blk_bias(int b) { return KV ? b >= 4 : (WN == 1 || b >= 4); }

template <bool KV, int WN> DI void k1_epilogue(const K1Args& A, LAS char* lds, f32x4 (&acc)[4][7], int panel, int hp, int wm);
template <bool KV, int WN>
DI void k1_body(const K1Args& A, LAS char* lds, int panel, int hp, int j32, int wm, int lane) {
  constexpr int NB = 7;
  const int wid = WN * 4 + wm;
#define STAMP_AT(i)
  const int G = hp >> 1, a = hp & 1;
  const int g4 = lane >> 4, l15 = lane & 15;
  const int c0 = (KV ? 1024 : 0) + 64 * hp;
  const int c1 = KV ? (2048 + 64 * hp) : (448 * G + 128 + 128 * a);
  const int c2 = KV ? (448 * G + 64 * a) : (448 * G + 128 + 128 * a + 64);
  const int c3 = 448 * G + 384 + (KV ? 0 : 32) + 16 * a;
  auto pcol = [&](int P) { return P == 0 ? c0 : P == 1 ? c1 : P == 2 ? c2 : c3; };
  f32x4 acc[4][NB];
  static_for<NB>([&](auto B) {
    constexpr int b = B;
    f32x4 init = {0.f, 0.f, 0.f, 0.f};
    if constexpr (blk_bias<KV, WN>(b)) {
      const u32x2 w = *(const u32x2*)((const char*)A.b1 + (pcol(blk_panel<KV, WN>(b)) + 16 * blk_cb<KV, WN>(b) + 4 * g4) * 2);
      init = (f32x4){bflo(w.x), bfhi(w.x), bflo(w.y), bfhi(w.y)};
    }
#pragma unroll
    for (int mb = 0; mb < 4; ++mb) acc[mb][b] = init;
  });
  const unsigned ldsb = (unsigned)(size_t)lds;
  unsigned xcur = ldsb + (64 * wm + l15) * 128 + ((g4 ^ ((lane >> 1) & 7)) * 16);
  const int q4 = l15 >> 2, p4 = lane & 3;
  const int sr = ((q4 >> 1) & 1) | ((g4 & 1) << 1);
  unsigned aw[4];
#pragma unroll
  for (int c = 0; c < 4; ++c) aw[c] = ldsb + K1_WOFF + (8 * g4 + q4) * 128 + ((c ^ sr) * 32) + p4 * 8;
  unsigned an[2];
#pragma unroll
  for (int t = 0; t < 2; ++t) an[t] = ldsb + K1_WOFF + 24576 + (8 * g4 + ((4 * t) ^ (4 * (g4 & 1))) + q4) * 32 + p4 * 8;

  bf16x8 Af[4]; s16x4 Bl[NB], Bh[NB];
  auto readA = [&](auto MB, unsigned addr) { rd128<MB * 2048>(Af[MB], addr); };
  auto readBh = [&](auto WP, auto KS, auto B, auto H) {
    constexpr int b = B, P = blk_panel<KV, WN>(b), c = blk_cb<KV, WN>(b), wo = WP * K1_WS;
    if constexpr (P < 3) { if constexpr (H == 0) rdtr<wo + P * 8192 + KS * 4096>(Bl[b], aw[c]); else rdtr<wo + P * 8192 + KS * 4096 + 512>(Bh[b], aw[c]); }
    else { if constexpr (H == 0) rdtr<wo + KS * 1024>(Bl[b], an[0]); else rdtr<wo + KS * 1024>(Bh[b], an[1]); }
  };
  auto mma = [&](auto B, auto MB) {
    constexpr int b = B, mb = MB;
    if constexpr (blk_std<KV, WN>(b)) acc[mb][b] = MFMA16(Af[mb], CAT(Bl[b], Bh[b]), acc[mb][b]);
    else acc[mb][b] = MFMA16(CAT(Bl[b], Bh[b]), Af[mb], acc[mb][b]);
  };
  using I0 = std::integral_constant<int, 0>; using I1 = std::integral_constant<int, 1>;
  using I2 = std::integral_constant<int, 2>; using I3 = std::integral_constant<int, 3>;
  using I5 = std::integral_constant<int, 5>; using I6 = std::integral_constant<int, 6>;
  auto h1_reads = [&](auto U, auto WP, auto KS, unsigned xa_) {
    constexpr int u = U;
    if constexpr (WN == 0) {
      if constexpr (u == 0) { readA(I0{}, xa_); readBh(WP, KS, I6{}, I0{}); }
      else if constexpr (u == 1) { readA(I1{}, xa_); readBh(WP, KS, I6{}, I1{}); }
      else readBh(WP, KS, std::integral_constant<int, ((u - 2) >> 1)>{}, std::integral_constant<int, (u & 1)>{});
    } else {
      if constexpr (u == 0) readA(I0{}, xa_);
      else if constexpr (u == 1) readA(I1{}, xa_);
      else if constexpr (u < 4) readBh(WP, KS, I6{}, std::integral_constant<int, (u & 1)>{});
      else readBh(WP, KS, std::integral_constant<int, ((u - 4) >> 1)>{}, std::integral_constant<int, (u & 1)>{});
    }
  };
  constexpr int L0 = WN == 0 ? 14 : 12, L1 = 26 - L0;
  auto step = [&](auto WP, auto KS, unsigned xcur, auto WPN, auto KSN, unsigned xnext, auto BARRIER) {
    static_for<L0>([&](auto S) {
      constexpr int s = S;
      if constexpr (s == 0 || s == 2 || s == 4) WAIT_L(8);
      if constexpr (s == 6) WAIT_L(6);
      if constexpr (s == 8) WAIT_L(4);
      if constexpr (s == 10) WAIT_L(2);
      if constexpr ((s & 1) == 0) SCHED();
      if constexpr (s < 12) mma(std::integral_constant<int, (s >> 1)>{}, std::integral_constant<int, (s & 1)>{}); else mma(I6{}, std::integral_constant<int, (s & 1)>{});
      if constexpr (s == 0) readBh(WP, KS, I5{}, I0{});
      if constexpr (s == 1) readBh(WP, KS, I5{}, I1{});
      if constexpr (s == 2) readA(I2{}, xcur);
      if constexpr (s == 3) readA(I3{}, xcur);
      SCHED();
    });
    WAIT_L(0);
    if constexpr (decltype(BARRIER)::value) {
      BAR();
      if constexpr (WN == 0) __builtin_amdgcn_s_setprio(2);
    }
    else { if constexpr (WN == 0) __builtin_amdgcn_s_setprio(0); }
    SCHED();
    static_for<L1>([&](auto U) {
      constexpr int u = U;
      if constexpr (WN == 0) mma(std::integral_constant<int, (u >> 1)>{}, std::integral_constant<int, 2 + (u & 1)>{});
      else { if constexpr (u < 2) mma(I6{}, std::integral_constant<int, 2 + u>{}); else mma(std::integral_constant<int, ((u - 2) >> 1)>{}, std::integral_constant<int, 2 + (u & 1)>{}); }
      h1_reads(U, WPN, KSN, xnext);
      SCHED();
    });
  };
  STAMP_AT(1);
  BAR(); SCHED();
  STAMP_AT(2);
  static_for<L1>([&](auto U) { h1_reads(U, I0{}, I0{}, xcur); });
  if constexpr (WN == 0) __builtin_amdgcn_s_setprio(2); else __builtin_amdgcn_s_setprio(1);
  int xs = 0;
  auto ktile = [&](auto WP, int t) {
    unsigned x1 = xcur ^ 64u, xn = xcur + (xs == 2 ? (unsigned)(-2 * K1_XS) : (unsigned)K1_XS);
    pin(x1); pin(xn);
    step(WP, I0{}, xcur, WP, I1{}, x1, std::false_type{});
    step(WP, I1{}, x1, std::integral_constant<int, WP ^ 1>{}, I0{}, xn, std::true_type{});
    xcur = xn; xs = xs == 2 ? 0 : xs + 1;
  };
#pragma unroll 1
  for (int t = 0; t < 16; t += 2) { ktile(I0{}, t); ktile(I1{}, t + 1); }
  SCHED(); WAIT_L(0); SCHED();
  __builtin_amdgcn_s_setprio(0);
  STAMP_AT(3);

  k1_epilogue<KV, WN>(A, lds, acc, panel, hp, wm);
  WAIT_V(0);
}

template <bool KV, int WN>
DI void k1_epilogue(const K1Args& A, LAS char* lds, f32x4 (&acc)[4][7], int panel, int hp, int wm) {
  int lane = threadIdx.x & 63; asm volatile("" : "+v"(lane));
  const int g4 = lane >> 4, l15 = lane & 15;
  const int G = hp >> 1, a = hp & 1;
  const int c1 = 448 * G + 128 + 128 * a;
  const int c2 = KV ? (448 * G + 64 * a) : (448 * G + 128 + 128 * a + 64);
  const rsrc_t rws = mk_rsrc(A.ws);
  const unsigned rowbase = (unsigned)panel * 256 + 64 * wm;
  const int chunk_g = (g4 & 1) * 2 + (g4 >> 1);
  auto pack2 = [&](const f32x4& e, const f32x4& o) {
    const unsigned e0 = cvt_pk(e[0], e[1]), e1 = cvt_pk(e[2], e[3]), o0 = cvt_pk(o[0], o[1]), o1 = cvt_pk(o[2], o[3]);
    const auto r0 = __builtin_amdgcn_permlane16_swap(e0, o0, false, false);
    const auto r1 = __builtin_amdgcn_permlane16_swap(e1, o1, false, false);
    return (u32x4){r0[0], r1[0], r0[1], r1[1]};
  };
  auto flush_rows = [&](LAS char* tile, int i0, int i1, int col0) {
#pragma unroll
    for (int i = i0; i < i1; ++i) {
      const u32x4 w = *(LAS u32x4*)(tile + i * 1024 + lane * 16);
      const int r = 8 * i + (lane >> 3), ch = (lane & 7) ^ (r & 7);
      __builtin_amdgcn_raw_buffer_store_b128(w, rws, (unsigned)(WS_XA + ((rowbase + r) * XA_LD + col0 + ch * 8) * 2), 0, SC1);
    }
  };
  LAS char* t2 = lds + wm * 8192;
  if constexpr (!KV && WN == 0) {
    LAS char* tq = lds + K2_SCR + wm * 8192;
#pragma unroll
    for (int mb = 0; mb < 4; ++mb)
#pragma unroll
      for (int p = 0; p < 2; ++p) *(LAS u32x4*)(tq + (16 * mb + l15) * 128 + (((4 * p + chunk_g) ^ (l15 & 7)) * 16)) = pack2(acc[mb][2 * p], acc[mb][2 * p + 1]);
  }
  LAS char* sc = lds + 2 * K1_XS;
  LAS char* sz = lds + K1_XS;
  if constexpr (KV) {
    f32x4 kvD[4]; f32x4 zD[2];
#pragma unroll
    for (int i = 0; i < 4; ++i) kvD[i] = (f32x4){0.f, 0.f, 0.f, 0.f};
    zD[0] = zD[1] = (f32x4){0.f, 0.f, 0.f, 0.f};
    const bf16x8 ones = {0x3f80, 0x3f80, 0x3f80, 0x3f80, 0x3f80, 0x3f80, 0x3f80, 0x3f80};
#pragma unroll
    for (int mp = 0; mp < 2; ++mp) {
      bf16x8 ka[2], vb[2];
#pragma unroll
      for (int b = 0; b < 2; ++b) {
        const f32x4 k0 = phi4(acc[2 * mp][b]), k1 = phi4(acc[2 * mp + 1][b]);
        const u32x4 w = {cvt_pk(k0[0], k0[1]), cvt_pk(k0[2], k0[3]), cvt_pk(k1[0], k1[1]), cvt_pk(k1[2], k1[3])};
        ka[b] = __builtin_bit_cast(bf16x8, w);
        const f32x4 v0 = acc[2 * mp][2 + b], v1 = acc[2 * mp + 1][2 + b];
        const u32x4 u = {cvt_pk(v0[0], v0[1]), cvt_pk(v0[2], v0[3]), cvt_pk(v1[0], v1[1]), cvt_pk(v1[2], v1[3])};
        vb[b] = __builtin_bit_cast(bf16x8, u);
      }
#pragma unroll
      for (int db = 0; db < 2; ++db) {
#pragma unroll
        for (int eb = 0; eb < 2; ++eb) kvD[db * 2 + eb] = MFMA16(ka[db], vb[eb], kvD[db * 2 + eb]);
        zD[db] = MFMA16(ka[db], ones, zD[db]);
      }
    }
#pragma unroll
    for (int i = 0; i < 4; ++i) *(LAS f32x4*)(sc + ((WN * 4 + wm) * 4 + i) * 1024 + lane * 16) = kvD[i];
    if (l15 == 0) {
#pragma unroll
      for (int db = 0; db < 2; ++db) *(LAS f32x4*)(sz + ((WN * 4 + wm) * 2 + db) * 64 + g4 * 16) = zD[db];
    }
  }
#pragma unroll
  for (int mb = 0; mb < 4; ++mb) *(LAS u32x4*)(t2 + (16 * mb + l15) * 128 + (((4 * WN + chunk_g) ^ (l15 & 7)) * 16)) = pack2(mish4(acc[mb][4]), mish4(acc[mb][5]));
#pragma unroll
  for (int m2 = 0; m2 < 2; ++m2) {
    const f32x4 v = mish4(acc[2 * WN + m2][6]);
    const u32x2 w = {cvt_pk(v[0], v[1]), cvt_pk(v[2], v[3])};
    __builtin_amdgcn_raw_buffer_store_b64(w, rws, (unsigned)(WS_NB + ((G * 4 + (KV ? 0 : 2) + a) * 2048 + rowbase + 16 * (2 * WN + m2) + l15) * 32 + g4 * 8), 0, SC1);
  }
  WAIT_L(0); BAR(); SCHED();
  flush_rows(t2, 4 * WN, 4 * WN + 4, c2);
  if constexpr (KV) {
    const int i4 = wm * 64 + lane, e = i4 >> 3, d0 = (i4 & 7) * 4;
    const int off = (((d0 >> 4) * 2 + (e >> 4)) * 64 + (e & 15) + 16 * ((d0 & 15) >> 2)) * 16;
    f32x4 s = {0.f, 0.f, 0.f, 0.f};
#pragma unroll
    for (int m = 0; m < 4; ++m) s += *(LAS f32x4*)(sc + (WN * 4 + m) * 4096 + off);
    __builtin_amdgcn_raw_buffer_store_b128(__builtin_bit_cast(u32x4, s), rws, (unsigned)(WS_KV + ((panel * 32 + 2 * hp + WN) * 1024 + i4 * 4) * 4), 0, SC1);
    if (wm == 0 && lane < 8) {
      const int db = lane >> 2, gq = lane & 3;
      f32x4 zs = {0.f, 0.f, 0.f, 0.f};
#pragma unroll
      for (int m = 0; m < 4; ++m) zs += *(LAS f32x4*)(sz + ((WN * 4 + m) * 2 + db) * 64 + gq * 16);
      __builtin_amdgcn_raw_buffer_store_b128(__builtin_bit_cast(u32x4, zs), rws, (unsigned)(WS_Z + ((panel * 32 + 2 * hp + WN) * 32 + 16 * db + 4 * gq) * 4), 0, SC1);
    }
  } else if constexpr (WN == 1) {
    LAS char* t1 = lds + K1_XS + wm * 8192;
#pragma unroll
    for (int mb = 0; mb < 4; ++mb) {
#pragma unroll
      for (int p = 0; p < 2; ++p) *(LAS u32x4*)(t1 + (16 * mb + l15) * 128 + (((4 * p + chunk_g) ^ (l15 & 7)) * 16)) = pack2(mish4(acc[mb][2 * p]), mish4(acc[mb][2 * p + 1]));
      flush_rows(t1, 2 * mb, 2 * mb + 2, c1);
    }
  }
}

DI void k2_compute(const K1Args& A, LAS char* lds, int tm, int tn, int role, int panel, bool isq, int hp, int wid) {
  int lane = threadIdx.x & 63; asm volatile("" : "+v"(lane));
  const int nt = role == 0 ? K2_NA + 16 : 56 - K2_NA, tile = tm * 8 + tn;
  const int wm = wid >> 2, wn = wid & 3;
  const int g4 = lane >> 4, l15 = lane & 15;
  BAR();
  bf16x8 pf[2][2];
  if (isq) {
#pragma unroll
    for (int mb = 0; mb < 2; ++mb)
#pragma unroll
      for (int h = 0; h < 2; ++h) {
        const int r = 32 * wid + 16 * mb + l15;
        const u32x4 raw = *(LAS u32x4*)(lds + K2_SCR + r * 128 + (((4 * h + g4) ^ (r & 7)) * 16));
        u32x4 w;
#pragma unroll
        for (int i = 0; i < 4; ++i) w[i] = cvt_pk(phi_f(bflo(raw[i])), phi_f(bfhi(raw[i])));
        pf[mb][h] = __builtin_bit_cast(bf16x8, w);
      }
  }
  BAR();
  const unsigned prow = (unsigned)panel * 256 + 32 * wid;
  const rsrc_t rws = mk_rsrc(A.ws);
  u32x4 part[8], zq; unsigned gv[2], gq[4];
  if (isq) {
#pragma unroll
    for (int q = 0; q < 8; ++q) part[q] = __builtin_amdgcn_raw_buffer_load_b128(rws, (unsigned)(WS_KV + ((q * 32 + 2 * hp) * 1024 + (wid * 64 + lane) * 4) * 4), 0, SC1);
    zq = __builtin_amdgcn_raw_buffer_load_b128(rws, (unsigned)(WS_Z + ((lane >> 3) & 7) * 4096 + (2 * hp * 32 + (wid & 1) * 32 + (lane & 7) * 4) * 4), 0, SC1);
#pragma unroll
    for (int mb = 0; mb < 2; ++mb) {
      const int r = 32 * wid + 16 * mb + l15;
      (void)r;
      gv[mb] = __builtin_amdgcn_raw_buffer_load_b32(rws, (unsigned)(WS_G + (prow + 16 * mb + l15) * 4), 0, SC1);
    }
  }
#pragma unroll
  for (int mb = 0; mb < 4; ++mb) gq[mb] = __builtin_amdgcn_raw_buffer_load_b32(rws, (unsigned)(WS_G + (tm * 128 + 64 * wm + 16 * mb + l15) * 4), 0, SC1);
  const unsigned ldsb = (unsigned)(size_t)lds;
  unsigned ax[2];
#pragma unroll
  for (int ks = 0; ks < 2; ++ks) ax[ks] = ldsb + (64 * wm + l15) * 128 + (((4 * ks + g4) ^ ((lane >> 1) & 7)) * 16);
  const int q4 = l15 >> 2, p4 = lane & 3;
  const int sr = ((q4 >> 1) & 1) | ((g4 & 1) << 1);
  unsigned aw[2];
#pragma unroll
  for (int nb = 0; nb < 2; ++nb) aw[nb] = ldsb + K2_WOFF + (wn >> 1) * 8192 + (8 * g4 + q4) * 128 + (((2 * (wn & 1) + nb) ^ sr) * 32) + p4 * 8;
  f32x4 acc[4][2];
#pragma unroll
  for (int i = 0; i < 4; ++i) acc[i][0] = acc[i][1] = (f32x4){0.f, 0.f, 0.f, 0.f};
  bf16x8 Af[2][2][4]; s16x4 Bl[2][2][2], Bh[2][2][2];
  auto rd = [&](auto BUF, auto I, const unsigned (&pa)[2], const unsigned (&pw)[2]) {
    constexpr int i = I, ks = i >> 3, r = i & 7;
    if constexpr (r < 2) { if constexpr (r == 0) rdtr<ks * 4096>(Bl[BUF][ks][0], pw[0]); else rdtr<ks * 4096 + 512>(Bh[BUF][ks][0], pw[0]); }
    else if constexpr (r < 6) rd128<(r - 2) * 2048>(Af[BUF][ks][r - 2], pa[ks]);
    else { if constexpr (r == 6) rdtr<ks * 4096>(Bl[BUF][ks][1], pw[1]); else rdtr<ks * 4096 + 512>(Bh[BUF][ks][1], pw[1]); }
  };
  using I0 = std::integral_constant<int, 0>; using I1 = std::integral_constant<int, 1>;
  LAS char* kvs = lds + K2_SCR;
  f32x4 ksum = {0.f, 0.f, 0.f, 0.f}, zsum = {0.f, 0.f, 0.f, 0.f};
  if (isq) {
#pragma unroll
    for (int q = 0; q < 8; ++q) ksum += __builtin_bit_cast(f32x4, part[q]);
    zsum = __builtin_bit_cast(f32x4, zq);
    if (wid < 2) {
#pragma unroll
      for (int o = 8; o <= 32; o <<= 1) { zsum[0] += __shfl_xor(zsum[0], o); zsum[1] += __shfl_xor(zsum[1], o); zsum[2] += __shfl_xor(zsum[2], o); zsum[3] += __shfl_xor(zsum[3], o); }
    }
    pin(pf[0][0]); pin(pf[0][1]); pin(pf[1][0]); pin(pf[1][1]);
  }
  WAIT_L(0);
  STAMP_AT(1);
  BAR(); SCHED();
  STAMP_AT(2);
  if (isq) {
    *(LAS u32x2*)(kvs + (wid * 64 + lane) * 8) = (u32x2){cvt_pk(ksum[0], ksum[1]), cvt_pk(ksum[2], ksum[3])};
    if (wid < 2 && lane < 8) *(LAS u32x2*)(kvs + 4096 + wid * 64 + lane * 8) = (u32x2){cvt_pk(zsum[0], zsum[1]), cvt_pk(zsum[2], zsum[3])};
  }
  SCHED();
  static_for<16>([&](auto I) { rd(I0{}, I, ax, aw); });
  auto body = [&](auto CUR, int i) {
    constexpr int cur = CUR;
    const unsigned so1 = ((i + 1) & 3) * K2_XS;
    unsigned pa[2] = {ax[0] + so1, ax[1] + so1}, pw[2] = {aw[0] + so1, aw[1] + so1};
    pin(pa[0]); pin(pa[1]); pin(pw[0]); pin(pw[1]);
    WAIT_L(0); BAR(); SCHED();
    static_for<16>([&](auto S) {
      constexpr int s = S, ks = s >> 3, nb = (s >> 2) & 1, mb = s & 3;
      acc[mb][nb] = MFMA16(CAT(Bl[cur][ks][nb], Bh[cur][ks][nb]), Af[cur][ks][mb], acc[mb][nb]);
      if constexpr (s < 8) { rd(std::integral_constant<int, cur ^ 1>{}, S, pa, pw); }
      else if constexpr (s < 12) { rd(std::integral_constant<int, cur ^ 1>{}, std::integral_constant<int, 2 * s - 8>{}, pa, pw); rd(std::integral_constant<int, cur ^ 1>{}, std::integral_constant<int, 2 * s - 7>{}, pa, pw); }
      SCHED();
    });
  };
  body(I0{}, 0);
  if (isq) {
    const unsigned kb = (unsigned)(size_t)kvs;
    const int chunk_g = (g4 & 1) * 2 + (g4 >> 1);
#pragma unroll
    for (int h = 0; h < 2; ++h) {
      bf16x8 ka[2], za;
      rd128<0>(ka[0], kb + h * 2048 + l15 * 64 + g4 * 16); rd128<1024>(ka[1], kb + h * 2048 + l15 * 64 + g4 * 16); rd128<4096>(za, kb + h * 64 + g4 * 16);
      WAIT_L(0); SCHED();
#pragma unroll
      for (int mb = 0; mb < 2; ++mb) {
        const f32x4 zero = {0.f, 0.f, 0.f, 0.f};
        const f32x4 n0 = MFMA16(ka[0], pf[mb][h], zero), n1 = MFMA16(ka[1], pf[mb][h], zero), dn = MFMA16(za, pf[mb][h], zero);
        const float sc = __uint_as_float(gv[mb]) * __builtin_amdgcn_rcpf(dn[0] + 1e-6f);
        const f32x4 e = n0 * sc, o = n1 * sc;
        const unsigned e0 = cvt_pk(e[0], e[1]), e1 = cvt_pk(e[2], e[3]), o0 = cvt_pk(o[0], o[1]), o1 = cvt_pk(o[2], o[3]);
        const auto r0 = __builtin_amdgcn_permlane16_swap(e0, o0, false, false);
        const auto r1 = __builtin_amdgcn_permlane16_swap(e1, o1, false, false);
        const u32x4 w = {r0[0], r1[0], r0[1], r1[1]};
        __builtin_amdgcn_raw_buffer_store_b128(w, rws, (unsigned)(WS_XA + ((prow + 16 * mb + l15) * XA_LD + 3584 + 64 * hp + 32 * h + chunk_g * 8) * 2), 0, SC1);
      }
    }
    SCHED();
  }
  body(I1{}, 1);
  body(I0{}, 2);
  float omg[4];
#pragma unroll
  for (int mb = 0; mb < 4; ++mb) omg[mb] = 1.f - __uint_as_float(gq[mb]);
  body(I1{}, 3);
  WAIT_V(0);
  WAIT_L(0); SCHED();
  const int n1 = role == 0 ? K2_NA : nt;
#pragma unroll 1
  for (int i = 4; i < n1; i += 2) { body(I0{}, i); body(I1{}, i + 1); }
  SCHED(); WAIT_L(0); SCHED();
#pragma unroll
  for (int mb = 0; mb < 4; ++mb) { acc[mb][0] *= omg[mb]; acc[mb][1] *= omg[mb]; }
#pragma unroll 1
  for (int i = n1; i < nt; i += 2) { body(I0{}, i); body(I1{}, i + 1); }
  SCHED(); WAIT_L(0); SCHED();
  STAMP_AT(3);
  if (role == 1) {
    const unsigned poff = (unsigned)(WS_PART + (tile * 8 + wid) * 4096 + lane * 16);
#pragma unroll
    for (int mb = 0; mb < 4; ++mb) {
      const u32x4 w = {cvt_pk(acc[mb][0][0], acc[mb][0][1]), cvt_pk(acc[mb][0][2], acc[mb][0][3]), cvt_pk(acc[mb][1][0], acc[mb][1][1]), cvt_pk(acc[mb][1][2], acc[mb][1][3])};
      __builtin_amdgcn_raw_buffer_store_b128(w, rws, poff + mb * 1024, 0, SC1);
    }
    STAMP_AT(4);
    WAIT_V(0);
    BAR();
  } else {
    BAR();
    BAR();
    STAMP_AT(4);
    const int chunk_g = (g4 & 1) * 2 + (g4 >> 1);
    LAS char* pl = lds + K2_SCR + wid * 4096 + lane * 16;
#pragma unroll
    for (int mb = 0; mb < 4; ++mb) {
      const u32x4 pw_ = *(LAS u32x4*)(pl + mb * 1024);
      const f32x4 e = acc[mb][0] + (f32x4){bflo(pw_[0]), bfhi(pw_[0]), bflo(pw_[1]), bfhi(pw_[1])}, o = acc[mb][1] + (f32x4){bflo(pw_[2]), bfhi(pw_[2]), bflo(pw_[3]), bfhi(pw_[3])};
      const unsigned e0 = cvt_pk(e[0], e[1]), e1 = cvt_pk(e[2], e[3]), o0 = cvt_pk(o[0], o[1]), o1 = cvt_pk(o[2], o[3]);
      const auto r0 = __builtin_amdgcn_permlane16_swap(e0, o0, false, false);
      const auto r1 = __builtin_amdgcn_permlane16_swap(e1, o1, false, false);
      const u32x4 w = {r0[0], r1[0], r0[1], r1[1]};
      __builtin_amdgcn_raw_buffer_store_b128(w, mk_rsrc(A.out), (unsigned)(((tm * 128 + 64 * wm + 16 * mb + l15) * 1024 + tn * 128 + 32 * wn + chunk_g * 8) * 2), 0, OUT_AUX);
    }
  }
}

DI void loader(const K1Args& A, LAS char* lds, bool isx, int lane) {
  const int b = blockIdx.x, xcd = b & 7, idx = b >> 3, rq = xcd >> 1, ch = xcd & 1;
  const int panel = 2 * rq + (idx >> 4), c16 = idx & 15, hp = 8 * ch + (c16 >> 1), j32 = ch * 16 + c16;
  const bool kv = (c16 & 1) != 0;
  u64 ep; poll_issue(ep, A.ws + WS_EP);
  const int G = hp >> 1, a = hp & 1;
  const int c0 = (kv ? 1024 : 0) + 64 * hp;
  const int c1 = kv ? (2048 + 64 * hp) : (448 * G + 128 + 128 * a);
  const int c2 = kv ? (448 * G + 64 * a) : (448 * G + 128 + 128 * a + 64);
  const int c3 = 448 * G + 384 + (kv ? 0 : 32) + 16 * a;
  unsigned chx[2], chw[2];
#pragma unroll
  for (int par = 0; par < 2; ++par) {
    chx[par] = (unsigned)(((lane & 7) ^ ((4 * par + (lane >> 4)) & 7)) * 16);
    chw[par] = (unsigned)(((lane & 7) ^ ((((lane >> 4) & 1) | (par << 1)) << 1)) * 16);
  }
  unsigned tdummy = 0;
  u64 tag;
  {
    unsigned vA[4], vB[4], vN[2];
#pragma unroll
    for (int k = 0; k < 4; ++k) {
      vA[k] = (isx ? (unsigned)((8 * k + (lane >> 3)) * 2048) + chx[k & 1] : (unsigned)((8 * k + (lane >> 3)) * 6144) + chw[k & 1]) + 3072 - k * 1024;
      vB[k] = (unsigned)((8 * k + (lane >> 3)) * 7168) + chw[k & 1] + 3072 - k * 1024;
    }
#pragma unroll
    for (int n = 0; n < 2; ++n) { const int rho = 32 * n + (lane >> 1); vN[n] = (unsigned)((rho ^ (((rho >> 3) & 1) << 2)) * 7168 + (lane & 1) * 16) + 3072 - n * 1024; }
    const rsrc_t rx = mk_rsrc((const char*)A.x - 3072), rqk = mk_rsrc((const char*)A.wqkv - 3072), r1 = mk_rsrc((const char*)A.w1 - 3072);
    const unsigned xrow0 = (unsigned)panel * 256 * 2048;
    auto issueX = [&](int t, int xs) {
#pragma unroll
      for (int g = 0; g < 8; ++g)
        static_for<4>([&](auto K) { __builtin_amdgcn_raw_ptr_buffer_load_lds(rx, (LAS void*)(lds + xs * K1_XS + g * 4096), 16, vA[K], xrow0 + t * 128 + g * 65536, K * 1024, 0); });
    };
    auto issueW = [&](int t) {
      LAS char* dst = lds + K1_WOFF + (t & 1) * K1_WS;
#pragma unroll
      for (int g = 0; g < 2; ++g)
        static_for<4>([&](auto K) { __builtin_amdgcn_raw_ptr_buffer_load_lds(rqk, (LAS void*)(dst + g * 4096), 16, vA[K], (t * 64 + g * 32) * 6144 + c0 * 2, K * 1024, 0); });
      if (kv) {
#pragma unroll
        for (int g = 0; g < 2; ++g)
          static_for<4>([&](auto K) { __builtin_amdgcn_raw_ptr_buffer_load_lds(rqk, (LAS void*)(dst + 8192 + g * 4096), 16, vA[K], (t * 64 + g * 32) * 6144 + c1 * 2, K * 1024, 0); });
      } else {
#pragma unroll
        for (int g = 0; g < 2; ++g)
          static_for<4>([&](auto K) { __builtin_amdgcn_raw_ptr_buffer_load_lds(r1, (LAS void*)(dst + 8192 + g * 4096), 16, vB[K], (t * 64 + g * 32) * 7168 + c1 * 2, K * 1024, 0); });
      }
#pragma unroll
      for (int g = 0; g < 2; ++g)
        static_for<4>([&](auto K) { __builtin_amdgcn_raw_ptr_buffer_load_lds(r1, (LAS void*)(dst + 16384 + g * 4096), 16, vB[K], (t * 64 + g * 32) * 7168 + c2 * 2, K * 1024, 0); });
      static_for<2>([&](auto N) { __builtin_amdgcn_raw_ptr_buffer_load_lds(r1, (LAS void*)(dst + 24576), 16, vN[N], t * 64 * 7168 + c3 * 2, N * 1024, 0); });
    };
    auto touch3 = [&]() {
      const int w = b * 2 + (isx ? 0 : 1);
#pragma unroll
      for (int n = 0; n < 3; ++n) {
        const int k = n * 64 + lane, li = w * 144 + (k < 144 ? k : 143);
        touch(tdummy, li < 57344 ? (const char*)A.w2 + (size_t)li * 128 : (const char*)A.wo + (size_t)(li - 57344) * 128);
      }
    };
    if (isx) {
      issueX(0, 0); issueX(1, 1);
#pragma unroll
      for (int n = 0; n < 4; ++n) touch(tdummy, (const char*)A.x + (size_t)(panel * 256 + 16 * c16 + 4 * n + (lane >> 4)) * 2048 + (lane & 15) * 128);
      auto wtouch = [&](int t0) {
        const int li = b * 52 + (lane < 52 ? lane : 51);
        int row = 64 * t0 + li / 104; row = row < 1024 ? row : 1023;
        const int c = li % 104;
        touch(tdummy, c < 48 ? (const char*)A.wqkv + (size_t)row * 6144 + c * 128 : (const char*)A.w1 + (size_t)row * 7168 + (c - 48) * 128);
      };
      auto wtouch1 = [&](int t0) {
        const int li = b * 26 + (lane < 26 ? lane : 25);
        const int row = 64 * (t0 < 16 ? t0 : 15) + li / 104, c = li % 104;
        touch(tdummy, c < 48 ? (const char*)A.wqkv + (size_t)row * 6144 + c * 128 : (const char*)A.w1 + (size_t)row * 7168 + (c - 48) * 128);
      };
#define WD1 4
      wtouch(2);
      WAIT_V(37); pin(ep); tag = ep + 1;
      BAR();
      if (lane == 0) __hip_atomic_store((gu64*)(A.ws + WS_START) + b, tag, RLX_AGENT);
      issueX(2, 2);
#pragma unroll
      for (int t = 4; t < WD1 + 1; ++t) wtouch1(t);
      int xs = 0;
#pragma unroll 1
      for (int t = 0; t < 13; ++t) {
        WAIT_V(33); BAR(); issueX(t + 3, xs); xs = xs == 2 ? 0 : xs + 1;
        wtouch1(t + 1 + WD1);
      }
      touch3();
      WAIT_V(35); BAR();
      WAIT_V(3); BAR();
      u32x4 gx[8][2], gg[2];
#pragma unroll
      for (int r = 0; r < 8; ++r) { const char* xr = (const char*)A.x + (size_t)(panel * 256 + j32 * 8 + r) * 2048 + lane * 32; gload4(gx[r][0], xr); gload4(gx[r][1], xr + 16); }
      gload4(gg[0], (const char*)A.gw + lane * 32); gload4(gg[1], (const char*)A.gw + lane * 32 + 16);
      BAR();
      BAR();
      WAIT_V(0);
      {
        pin(gg[0]); pin(gg[1]);
        float s[8];
#pragma unroll
        for (int r = 0; r < 8; ++r) {
          pin(gx[r][0]); pin(gx[r][1]);
          s[r] = 0.f;
#pragma unroll
          for (int h = 0; h < 2; ++h)
#pragma unroll
            for (int i = 0; i < 4; ++i) { const unsigned ux = gx[r][h][i], ug = gg[h][i]; s[r] = __builtin_amdgcn_fdot2_f32_bf16(__builtin_bit_cast(bf16x2_t, ux), __builtin_bit_cast(bf16x2_t, ug), s[r], false); }
        }
#pragma unroll
        for (int o = 32; o >= 1; o >>= 1)
#pragma unroll
          for (int r = 0; r < 8; ++r) s[r] += __shfl_xor(s[r], o);
        float mine = s[0];
#pragma unroll
        for (int r = 1; r < 8; ++r) mine = lane == r ? s[r] : mine;
        const float gval = __builtin_amdgcn_rcpf(1.f + __builtin_amdgcn_exp2f(-LOG2E * mine));
        if (lane < 8) __hip_atomic_store((__attribute__((address_space(1))) unsigned*)(A.ws + WS_G) + panel * 256 + j32 * 8 + lane, __float_as_uint(gval), RLX_AGENT);
      }
    } else {
      issueW(0); issueW(1);
      WAIT_V(26); pin(ep); tag = ep + 1;
      BAR();
#pragma unroll 1
      for (int t = 0; t < 14; ++t) { WAIT_V(0); BAR(); issueW(t + 2); }
      touch3();
      WAIT_V(3); BAR();
      BAR();
    }
  }
  const int role = (idx >> 1) & 1, tm = 4 * rq + (idx >> 3), tn = 4 * ch + 2 * ((idx >> 2) & 1) + (idx & 1), tile = tm * 8 + tn;
  const int nt = role == 0 ? K2_NA + 16 : 56 - K2_NA;
  unsigned voff[4];
#pragma unroll
  for (int k = 0; k < 4; ++k)
    voff[k] = (isx ? (unsigned)((8 * k + (lane >> 3)) * (XA_LD * 2) + chx[k & 1]) : (unsigned)((8 * k + (lane >> 3)) * 2048 + chw[k & 1])) + 3072 - k * 1024;
  const unsigned gstep = isx ? 32 * XA_LD * 2 : 32 * 2048;
  auto issue = [&](rsrc_t r, unsigned so, int slot) {
    LAS char* st = lds + (isx ? 0 : K2_WOFF) + slot * K2_XS;
#pragma unroll
    for (int g = 0; g < 4; ++g) {
      const unsigned sg = isx ? so + g * gstep : so + (g & 1) * gstep + (g >> 1) * 128;
      static_for<4>([&](auto K) { __builtin_amdgcn_raw_ptr_buffer_load_lds(r, (LAS void*)(st + g * 4096), 16, voff[K], sg, K * 1024, SC1); });
    }
  };
  unsigned voffN[4];
#pragma unroll
  for (int k = 0; k < 4; ++k) { const unsigned c = chx[k & 1] >> 4; voffN[k] = (c >> 1) * 65536 + (8 * k + (lane >> 3)) * 32 + (c & 1) * 16 + 3072 - k * 1024; }
  const rsrc_t rn = mk_rsrc((const char*)(A.ws + WS_NB) - 3072);
  auto issueN = [&](int grp, int slot) {
    LAS char* st = lds + slot * K2_XS;
#pragma unroll
    for (int g = 0; g < 4; ++g)
      static_for<4>([&](auto K) { __builtin_amdgcn_raw_ptr_buffer_load_lds(rn, (LAS void*)(st + g * 4096), 16, voffN[K], (unsigned)grp * 262144 + (unsigned)(tm * 128 + g * 32) * 32, K * 1024, SC1); });
  };
  const rsrc_t r0 = mk_rsrc(isx ? (const char*)(A.ws + WS_XA) - 3072 : (const char*)A.w2 - 3072);
  const rsrc_t r1 = mk_rsrc(isx ? (const char*)(A.ws + WS_XA) - 3072 : (const char*)A.wo - 3072);
  const unsigned tstep = isx ? 128u : 64u * 2048u;
  const unsigned sbase = isx ? (unsigned)tm * 128 * (XA_LD * 2) : (unsigned)tn * 256;
  const unsigned s0 = sbase + (role == 0 ? 0 : K2_NA) * tstep;
  const unsigned s1 = isx ? sbase + 56 * tstep : sbase;
  const rsrc_t rp = mk_rsrc(A.ws + WS_PART);
  auto issue_partial = [&]() {
    LAS char* dst = lds + K2_SCR + (isx ? 0 : 16384);
#pragma unroll
    for (int g = 0; g < 4; ++g)
      static_for<4>([&](auto K) { __builtin_amdgcn_raw_ptr_buffer_load_lds(rp, (LAS void*)(dst + g * 4096), 16, lane * 16, (unsigned)tile * 32768 + (isx ? 0 : 16384) + g * 4096, K * 1024, SC1); });
  };
  auto issueA = [&](int pos) {
    const int kt = (role == 0 ? 0 : K2_NA) + pos, grp = kt / 7;
    if (isx && kt - 7 * grp == 6) issueN(grp, pos & 3); else issue(r0, s0 + pos * tstep, pos & 3);
  };
  if (!isx) { issueA(0); issueA(1); issueA(2); issueA(3);
  }
  if (!isx) BAR();
  if (isx) WAIT_V(0);
  BAR();
  if (isx) {
    if (lane == 0) __hip_atomic_store((gu64*)(A.ws + WS_DONE1) + panel * 32 + j32, tag, RLX_AGENT);
    const int slot = lane < 32 ? panel * 32 + lane : ((lane < 40 && !kv) ? (lane - 32) * 32 + (j32 | 1) : panel * 32);
    wait_all(A.ws + WS_DONE1 + (size_t)slot * 8, tag);
  }
  BAR();
  if (isx) { issueA(0); issueA(1); issueA(2); WAIT_V(32); } else WAIT_V(48);
  BAR();
  if (isx) issueA(3);
  auto run = [&](int i0, int i1, rsrc_t r, unsigned so) {
#pragma unroll 1
    for (int i = i0; i < i1; ++i, so += tstep) { WAIT_V(32); BAR(); issue(r, so, i & 3); }
  };
  auto runA = [&](int i0, int i1) {
#pragma unroll 1
    for (int i = i0; i < i1; ++i) { WAIT_V(32); BAR(); issueA(i + 4); }
  };
  u64 pv = 0;
  runA(0, 5);
  if (isx && !kv && lane == 0) __hip_atomic_store((gu64*)(A.ws + WS_ODONE) + panel * 16 + hp, tag, RLX_AGENT);
  if (role == 0) {
    runA(5, K2_NA - 8);
    const char* od = A.ws + WS_ODONE + (size_t)(panel * 16 + (lane & 15)) * 8;
    if (isx) poll_issue(pv, (const void*)od);
    runA(K2_NA - 8, K2_NA - 4);
    if (isx) { WAIT_V(32); pin(pv); if (!__all(pv == tag)) wait_all((const void*)od, tag); }
    run(K2_NA - 4, nt - 4, r1, s1);
  } else runA(5, nt - 4);
  const char* pd = A.ws + WS_PDONE + (size_t)tile * 128;
  poll_issue(pv, (const void*)pd);
  WAIT_V(33); BAR();
  WAIT_V(17); BAR();
  WAIT_V(0);
  bool early = false;
  if (role == 0) { pin(pv); early = __all(pv == tag); }
  BAR();
  if (early) issue_partial();
  BAR();
  if (role == 0) {
    if (!early) wait_all((const void*)pd, tag);
    BAR();
    if (!early) issue_partial();
    WAIT_V(0);
    BAR();
  } else {
    BAR();
    if (isx && lane == 0) __hip_atomic_store((gu64*)(A.ws + WS_PDONE + (size_t)tile * 128), tag, RLX_AGENT);
  }
  if (b == 0 && isx) {
#pragma unroll 1
    for (int k = 0; k < 4; ++k) wait_all(A.ws + WS_START + (size_t)(k * 64 + lane) * 8, tag);
    if (lane == 0) __hip_atomic_store((gu64*)(A.ws + WS_EP), tag, RLX_AGENT);
  }
  WAIT_V(0); keep(tdummy);
}

__global__ __launch_bounds__(640, 3) void fused_kernel(K1Args A) {
  extern __shared__ __attribute__((aligned(16))) char smem[];
  LAS char* lds = (LAS char*)smem;
  const int b = blockIdx.x, xcd = b & 7, idx = b >> 3, rq = xcd >> 1, ch = xcd & 1;
  const int panel = 2 * rq + (idx >> 4), c16 = idx & 15, hp = 8 * ch + (c16 >> 1), j32 = ch * 16 + c16;
  const int wid = __builtin_amdgcn_readfirstlane(threadIdx.x >> 6), lane = threadIdx.x & 63;
  if (wid >= 8) { loader(A, lds, wid == 8, lane); return; }
  if (c16 & 1) {
    if (wid < 4) k1_body<true, 0>(A, lds, panel, hp, j32, wid, lane); else k1_body<true, 1>(A, lds, panel, hp, j32, wid - 4, lane);
  } else {
    if (wid < 4) k1_body<false, 0>(A, lds, panel, hp, j32, wid, lane); else k1_body<false, 1>(A, lds, panel, hp, j32, wid - 4, lane);
  }
  k2_compute(A, lds, 4 * rq + (idx >> 3), 4 * ch + 2 * ((idx >> 2) & 1) + (idx & 1), (idx >> 1) & 1, panel, (c16 & 1) == 0, hp, wid);
}

extern "C" void kernel_launch(void* const* d_in, const int* in_sizes, int n_in, void* d_out, int out_size, void* d_ws, size_t ws_size, hipStream_t stream) {
  K1Args a; a.x = (const u16*)d_in[0]; a.wqkv = (const u16*)d_in[1]; a.w1 = (const u16*)d_in[4]; a.b1 = (const u16*)d_in[5]; a.gw = (const u16*)d_in[7]; a.ws = (char*)d_ws;
  a.w2 = (const u16*)d_in[6]; a.wo = (const u16*)d_in[2]; a.out = (u16*)d_out;
  (void)hipFuncSetAttribute((const void*)fused_kernel, hipFuncAttributeMaxDynamicSharedMemorySize, LDS_BYTES);
  fused_kernel<<<256, 640, LDS_BYTES, stream>>>(a);
}
```

```cpp
#include <hip/hip_runtime.h>
#include <stdint.h>
#include <utility>
#define LAS __attribute__((address_space(3)))
#define DI __device__ __forceinline__
typedef unsigned short u16;
typedef short bf16x8 __attribute__((ext_vector_type(8)));
typedef short s16x4 __attribute__((ext_vector_type(4)));
typedef float f32x4 __attribute__((ext_vector_type(4)));
typedef float f32x2 __attribute__((ext_vector_type(2)));
typedef unsigned u32x4 __attribute__((ext_vector_type(4)));
typedef unsigned u32x2 __attribute__((ext_vector_type(2)));
typedef __amdgpu_buffer_rsrc_t rsrc_t;

constexpr int XA_LD = 4608;
constexpr size_t WS_XA = 0;
constexpr size_t WS_KV = WS_XA + (size_t)2048 * XA_LD * 2;
constexpr size_t WS_Z  = WS_KV + (size_t)8 * 32 * 1024 * 4;
constexpr size_t WS_G  = WS_Z + (size_t)8 * 32 * 32 * 4;
constexpr size_t WS_EP    = WS_G + 2048 * 4;
constexpr size_t WS_DONE1 = WS_EP + 256;
constexpr size_t WS_ODONE = WS_DONE1 + 2048;
constexpr size_t WS_PDONE = WS_ODONE + 2048;
constexpr size_t WS_START = WS_PDONE + 128 * 128;
constexpr size_t WS_PART  = WS_START + 4096;
constexpr size_t WS_NB    = WS_PART + 128 * 32768;

#define SCHED() __builtin_amdgcn_sched_barrier(0)
#define WAIT_V(n) asm volatile("s_waitcnt vmcnt(%0)" ::"n"(n) : "memory")
#define WAIT_L(n) asm volatile("s_waitcnt lgkmcnt(%0)" ::"n"(n) : "memory")
#define BAR() __builtin_amdgcn_s_barrier()

template <int OFF> DI void rd128(bf16x8& d, unsigned a) { asm volatile("ds_read_b128 %0, %1 offset:%2" : "=v"(d) : "v"(a), "i"(OFF) : "memory"); }
template <int OFF> DI void rdtr(s16x4& d, unsigned a) { asm volatile("ds_read_b64_tr_b16 %0, %1 offset:%2" : "=v"(d) : "v"(a), "i"(OFF) : "memory"); }
typedef __bf16 bf16x2_t __attribute__((ext_vector_type(2)));
DI void gload4(u32x4& d, const void* p) { asm volatile("global_load_dwordx4 %0, %1, off" : "=v"(d) : "v"(p) : "memory"); }
template <class T> DI void pin(T& v) { asm volatile("" : "+v"(v)); }
template <class T> DI void keep(const T& v) { asm volatile("" :: "v"(v)); }
DI int fresh_lane() { int l; asm volatile("v_mbcnt_lo_u32_b32 %0, -1, 0\n\tv_mbcnt_hi_u32_b32 %0, -1, %0" : "=v"(l)); return l; }
DI rsrc_t mk_rsrc(const void* p, unsigned n = 0x7fffffff) { return __builtin_amdgcn_make_buffer_rsrc((void*)p, 0, n, 0x00020000); }
DI unsigned cvt_pk(float lo, float hi) { return __builtin_bit_cast(unsigned, __builtin_convertvector((f32x2){lo, hi}, bf16x2_t)); }
DI float bflo(unsigned w) { return __uint_as_float(w << 16); }
DI float bfhi(unsigned w) { return __uint_as_float(w & 0xffff0000u); }
constexpr float LOG2E = 1.44269504088896f;
DI float phi_f(float t) { const float e = __builtin_amdgcn_exp2f(__builtin_fminf(t * LOG2E, 0.f)); return e + __builtin_fmaxf(t + 1e-6f, 1e-6f); }
DI f32x2 mish2(f32x2 h) {
  const f32x2 t = h * LOG2E;
  f32x2 e; e.x = __builtin_amdgcn_exp2f(t.x); e.y = __builtin_amdgcn_exp2f(t.y);
  const f32x2 a = __builtin_elementwise_fma(e, (f32x2){0.5f, 0.5f}, (f32x2){1.f, 1.f});
  const f32x2 d = __builtin_elementwise_fma(e, a, (f32x2){1.f, 1.f});
  f32x2 r; r.x = __builtin_amdgcn_rcpf(d.x); r.y = __builtin_amdgcn_rcpf(d.y);
  return __builtin_elementwise_fma(-h, r, h);
}
DI f32x4 mish4(const f32x4& v) { const f32x2 a = mish2((f32x2){v[0], v[1]}), b = mish2((f32x2){v[2], v[3]}); return (f32x4){a.x, a.y, b.x, b.y}; }
DI f32x4 phi4(const f32x4& v) { return (f32x4){phi_f(v[0]), phi_f(v[1]), phi_f(v[2]), phi_f(v[3])}; }
#define MFMA16(a, b, c) __builtin_amdgcn_mfma_f32_16x16x32_bf16((a), (b), (c), 0, 0, 0)
#define CAT(L, H) __builtin_shufflevector((L), (H), 0, 1, 2, 3, 4, 5, 6, 7)
template <class F, int... I> DI void static_for_impl(F&& f, std::integer_sequence<int, I...>) { (f(std::integral_constant<int, I>{}), ...); }
template <int N, class F> DI void static_for(F&& f) { static_for_impl(f, std::make_integer_sequence<int, N>{}); }

constexpr int K1_XS = 32768, K1_WS = 26624, K1_WOFF = 3 * K1_XS;
constexpr int K2_XS = 16384, K2_WOFF = 98304, K2_SCR = 65536, LDS_BYTES = 163840;
constexpr int K2_CHK = 2;
constexpr int K2_NA = 25;

static_assert(K2_CHK + 7 <= K2_NA - 8, "o' must be published before its consumers poll");

struct Args {
  const u16* x; const u16* wqkv; const u16* w1; const u16* b1; const u16* gw; char* ws; const u16* w2; const u16* wo; u16* out;
};
typedef unsigned long long u64;
typedef __attribute__((address_space(1))) u64 gu64;
#define RLX_AGENT __ATOMIC_RELAXED, __HIP_MEMORY_SCOPE_AGENT
constexpr int SC1 = 16;
DI void touch(unsigned& dummy, const void* p) { asm volatile("global_load_dword %0, %1, off" : "+v"(dummy) : "v"(p) : "memory"); }
DI void poll_issue(u64& d, const void* p) { asm volatile("global_load_dwordx2 %0, %1, off sc1" : "=v"(d) : "v"(p) : "memory"); }
DI void wait_all(const void* p, u64 tag) {
  for (unsigned spins = 0; spins < (1u << 20); ++spins) {
    const u64 v = __hip_atomic_load((const gu64*)p, RLX_AGENT);
    if (__all(v == tag)) return;
    __builtin_amdgcn_s_sleep(1);
  }
}

template <bool KV, int WN> constexpr int blk_panel(int b) { return b == 6 ? 3 : (KV ? (b >> 1) : (b < 4 ? WN : 2)); }
template <bool KV, int WN> constexpr int blk_cb(int b) { return b == 6 ? 0 : (KV ? 2 * WN + (b & 1) : (b < 4 ? b : 2 * WN + (b & 1))); }
template <bool KV, int WN> constexpr bool blk_std(int b) { return KV && b < 4; }
template <bool KV, int WN> constexpr bool blk_bias(int b) { return KV ? b >= 4 : (WN == 1 || b >= 4); }

template <bool KV, int WN> DI void k1_epilogue(const Args& A, LAS char* lds, f32x4 (&acc)[4][7], int panel, int hp, int wm);
template <bool KV, int WN>
DI void k1_body(const Args& A, LAS char* lds, int panel, int hp, int wm, int lane) {
  constexpr int NB = 7;
  const int G = hp >> 1, a = hp & 1;
  const int g4 = lane >> 4, l15 = lane & 15;
  const int c0 = (KV ? 1024 : 0) + 64 * hp;
  const int c1 = KV ? (2048 + 64 * hp) : (448 * G + 128 + 128 * a);
  const int c2 = KV ? (448 * G + 64 * a) : (448 * G + 128 + 128 * a + 64);
  const int c3 = 448 * G + 384 + (KV ? 0 : 32) + 16 * a;
  auto pcol = [&](int P) { return P == 0 ? c0 : P == 1 ? c1 : P == 2 ? c2 : c3; };
  f32x4 acc[4][NB];
  static_for<NB>([&](auto B) {
    constexpr int b = B;
    f32x4 init = {0.f, 0.f, 0.f, 0.f};
    if constexpr (blk_bias<KV, WN>(b)) {
      const u32x2 w = *(const u32x2*)((const char*)A.b1 + (pcol(blk_panel<KV, WN>(b)) + 16 * blk_cb<KV, WN>(b) + 4 * g4) * 2);
      init = (f32x4){bflo(w.x), bfhi(w.x), bflo(w.y), bfhi(w.y)};
    }
#pragma unroll
    for (int mb = 0; mb < 4; ++mb) acc[mb][b] = init;
  });
  const unsigned ldsb = (unsigned)(size_t)lds;
  unsigned xcur = ldsb + (64 * wm + l15) * 128 + ((g4 ^ ((lane >> 1) & 7)) * 16);
  const int q4 = l15 >> 2, p4 = lane & 3;
  const int sr = ((q4 >> 1) & 1) | ((g4 & 1) << 1);
  unsigned aw[4];
#pragma unroll
  for (int c = 0; c < 4; ++c) aw[c] = ldsb + K1_WOFF + (8 * g4 + q4) * 128 + ((c ^ sr) * 32) + p4 * 8;
  unsigned an[2];
#pragma unroll
  for (int t = 0; t < 2; ++t) an[t] = ldsb + K1_WOFF + 24576 + (8 * g4 + ((4 * t) ^ (4 * (g4 & 1))) + q4) * 32 + p4 * 8;

  bf16x8 Af[4]; s16x4 Bl[NB], Bh[NB];
  auto readA = [&](auto MB, unsigned addr) { rd128<MB * 2048>(Af[MB], addr); };
  auto readBh = [&](auto WP, auto KS, auto B, auto H) {
    constexpr int b = B, P = blk_panel<KV, WN>(b), c = blk_cb<KV, WN>(b), wo = WP * K1_WS;
    if constexpr (P < 3) { if constexpr (H == 0) rdtr<wo + P * 8192 + KS * 4096>(Bl[b], aw[c]); else rdtr<wo + P * 8192 + KS * 4096 + 512>(Bh[b], aw[c]); }
    else { if constexpr (H == 0) rdtr<wo + KS * 1024>(Bl[b], an[0]); else rdtr<wo + KS * 1024>(Bh[b], an[1]); }
  };
  auto mma = [&](auto B, auto MB) {
    constexpr int b = B, mb = MB;
    if constexpr (blk_std<KV, WN>(b)) acc[mb][b] = MFMA16(Af[mb], CAT(Bl[b], Bh[b]), acc[mb][b]);
    else acc[mb][b] = MFMA16(CAT(Bl[b], Bh[b]), Af[mb], acc[mb][b]);
  };
  using I0 = std::integral_constant<int, 0>; using I1 = std::integral_constant<int, 1>;
  using I2 = std::integral_constant<int, 2>; using I3 = std::integral_constant<int, 3>;
  using I5 = std::integral_constant<int, 5>; using I6 = std::integral_constant<int, 6>;
  auto h1_reads = [&](auto U, auto WP, auto KS, unsigned xa_) {
    constexpr int u = U;
    if constexpr (WN == 0) {
      if constexpr (u == 0) { readA(I0{}, xa_); readBh(WP, KS, I6{}, I0{}); }
      else if constexpr (u == 1) { readA(I1{}, xa_); readBh(WP, KS, I6{}, I1{}); }
      else readBh(WP, KS, std::integral_constant<int, ((u - 2) >> 1)>{}, std::integral_constant<int, (u & 1)>{});
    } else {
      if constexpr (u == 0) readA(I0{}, xa_);
      else if constexpr (u == 1) readA(I1{}, xa_);
      else if constexpr (u < 4) readBh(WP, KS, I6{}, std::integral_constant<int, (u & 1)>{});
      else readBh(WP, KS, std::integral_constant<int, ((u - 4) >> 1)>{}, std::integral_constant<int, (u & 1)>{});
    }
  };
  constexpr int L0 = WN == 0 ? 14 : 12, L1 = 26 - L0;
  auto step = [&](auto WP, auto KS, unsigned xcur, auto WPN, auto KSN, unsigned xnext, auto BARRIER) {
    static_for<L0>([&](auto S) {
      constexpr int s = S;
      if constexpr (s == 0 || s == 2 || s == 4) WAIT_L(8);
      if constexpr (s == 6) WAIT_L(6);
      if constexpr (s == 8) WAIT_L(4);
      if constexpr (s == 10) WAIT_L(2);
      if constexpr ((s & 1) == 0) SCHED();
      if constexpr (s < 12) mma(std::integral_constant<int, (s >> 1)>{}, std::integral_constant<int, (s & 1)>{}); else mma(I6{}, std::integral_constant<int, (s & 1)>{});
      if constexpr (s == 0) readBh(WP, KS, I5{}, I0{});
      if constexpr (s == 1) readBh(WP, KS, I5{}, I1{});
      if constexpr (s == 2) readA(I2{}, xcur);
      if constexpr (s == 3) readA(I3{}, xcur);
      SCHED();
    });
    WAIT_L(0);
    if constexpr (decltype(BARRIER)::value) {
      BAR();
      if constexpr (WN == 0) __builtin_amdgcn_s_setprio(2);
    }
    else { if constexpr (WN == 0) __builtin_amdgcn_s_setprio(0); }
    SCHED();
    static_for<L1>([&](auto U) {
      constexpr int u = U;
      if constexpr (WN == 0) mma(std::integral_constant<int, (u >> 1)>{}, std::integral_constant<int, 2 + (u & 1)>{});
      else { if constexpr (u < 2) mma(I6{}, std::integral_constant<int, 2 + u>{}); else mma(std::integral_constant<int, ((u - 2) >> 1)>{}, std::integral_constant<int, 2 + (u & 1)>{}); }
      h1_reads(U, WPN, KSN, xnext);
      SCHED();
    });
  };
  BAR(); SCHED();
  static_for<L1>([&](auto U) { h1_reads(U, I0{}, I0{}, xcur); });
  if constexpr (WN == 0) __builtin_amdgcn_s_setprio(2); else __builtin_amdgcn_s_setprio(1);
  int xs = 0;
  auto ktile = [&](auto WP) {
    unsigned x1 = xcur ^ 64u, xn = xcur + (xs == 2 ? (unsigned)(-2 * K1_XS) : (unsigned)K1_XS);
    pin(x1); pin(xn);
    step(WP, I0{}, xcur, WP, I1{}, x1, std::false_type{});
    step(WP, I1{}, x1, std::integral_constant<int, WP ^ 1>{}, I0{}, xn, std::true_type{});
    xcur = xn; xs = xs == 2 ? 0 : xs + 1;
  };
#pragma unroll 1
  for (int t = 0; t < 16; t += 2) { ktile(I0{}); ktile(I1{}); }
  SCHED(); WAIT_L(0); SCHED();
  __builtin_amdgcn_s_setprio(0);

  k1_epilogue<KV, WN>(A, lds, acc, panel, hp, wm);
  WAIT_V(0);
}

template <bool KV, int WN>
DI void k1_epilogue(const Args& A, LAS char* lds, f32x4 (&acc)[4][7], int panel, int hp, int wm) {
  int lane = fresh_lane();
  const int g4 = lane >> 4, l15 = lane & 15;
  const int G = hp >> 1, a = hp & 1;
  const int c1 = 448 * G + 128 + 128 * a;
  const int c2 = KV ? (448 * G + 64 * a) : (448 * G + 128 + 128 * a + 64);
  const rsrc_t rws = mk_rsrc(A.ws);
  const unsigned rowbase = (unsigned)panel * 256 + 64 * wm;
  const int chunk_g = (g4 & 1) * 2 + (g4 >> 1);
  auto pack2 = [&](const f32x4& e, const f32x4& o) {
    const unsigned e0 = cvt_pk(e[0], e[1]), e1 = cvt_pk(e[2], e[3]), o0 = cvt_pk(o[0], o[1]), o1 = cvt_pk(o[2], o[3]);
    const auto r0 = __builtin_amdgcn_permlane16_swap(e0, o0, false, false);
    const auto r1 = __builtin_amdgcn_permlane16_swap(e1, o1, false, false);
    return (u32x4){r0[0], r1[0], r0[1], r1[1]};
  };
  auto flush_rows = [&](LAS char* tile, int i0, int i1, int col0) {
#pragma unroll
    for (int i = i0; i < i1; ++i) {
      const u32x4 w = *(LAS u32x4*)(tile + i * 1024 + lane * 16);
      const int r = 8 * i + (lane >> 3), ch = (lane & 7) ^ (r & 7);
      __builtin_amdgcn_raw_buffer_store_b128(w, rws, (unsigned)(WS_XA + ((rowbase + r) * XA_LD + col0 + ch * 8) * 2), 0, SC1);
    }
  };
  LAS char* t2 = lds + wm * 8192;
  if constexpr (!KV && WN == 0) {
    LAS char* tq = lds + K2_SCR + wm * 8192;
#pragma unroll
    for (int mb = 0; mb < 4; ++mb)
#pragma unroll
      for (int p = 0; p < 2; ++p) *(LAS u32x4*)(tq + (16 * mb + l15) * 128 + (((4 * p + chunk_g) ^ (l15 & 7)) * 16)) = pack2(acc[mb][2 * p], acc[mb][2 * p + 1]);
  }
  LAS char* sc = lds + 2 * K1_XS;
  LAS char* sz = lds + K1_XS;
  if constexpr (KV) {
    f32x4 kvD[4]; f32x4 zD[2];
#pragma unroll
    for (int i = 0; i < 4; ++i) kvD[i] = (f32x4){0.f, 0.f, 0.f, 0.f};
    zD[0] = zD[1] = (f32x4){0.f, 0.f, 0.f, 0.f};
    const bf16x8 ones = {0x3f80, 0x3f80, 0x3f80, 0x3f80, 0x3f80, 0x3f80, 0x3f80, 0x3f80};
#pragma unroll
    for (int mp = 0; mp < 2; ++mp) {
      bf16x8 ka[2], vb[2];
#pragma unroll
      for (int b = 0; b < 2; ++b) {
        const f32x4 k0 = phi4(acc[2 * mp][b]), k1 = phi4(acc[2 * mp + 1][b]);
        const u32x4 w = {cvt_pk(k0[0], k0[1]), cvt_pk(k0[2], k0[3]), cvt_pk(k1[0], k1[1]), cvt_pk(k1[2], k1[3])};
        ka[b] = __builtin_bit_cast(bf16x8, w);
        const f32x4 v0 = acc[2 * mp][2 + b], v1 = acc[2 * mp + 1][2 + b];
        const u32x4 u = {cvt_pk(v0[0], v0[1]), cvt_pk(v0[2], v0[3]), cvt_pk(v1[0], v1[1]), cvt_pk(v1[2], v1[3])};
        vb[b] = __builtin_bit_cast(bf16x8, u);
      }
#pragma unroll
      for (int db = 0; db < 2; ++db) {
#pragma unroll
        for (int eb = 0; eb < 2; ++eb) kvD[db * 2 + eb] = MFMA16(ka[db], vb[eb], kvD[db * 2 + eb]);
        zD[db] = MFMA16(ka[db], ones, zD[db]);
      }
    }
#pragma unroll
    for (int i = 0; i < 4; ++i) *(LAS f32x4*)(sc + ((WN * 4 + wm) * 4 + i) * 1024 + lane * 16) = kvD[i];
    if (l15 == 0) {
#pragma unroll
      for (int db = 0; db < 2; ++db) *(LAS f32x4*)(sz + ((WN * 4 + wm) * 2 + db) * 64 + g4 * 16) = zD[db];
    }
  }
#pragma unroll
  for (int mb = 0; mb < 4; ++mb) *(LAS u32x4*)(t2 + (16 * mb + l15) * 128 + (((4 * WN + chunk_g) ^ (l15 & 7)) * 16)) = pack2(mish4(acc[mb][4]), mish4(acc[mb][5]));
#pragma unroll
  for (int m2 = 0; m2 < 2; ++m2) {
    const f32x4 v = mish4(acc[2 * WN + m2][6]);
    const u32x2 w = {cvt_pk(v[0], v[1]), cvt_pk(v[2], v[3])};
    __builtin_amdgcn_raw_buffer_store_b64(w, rws, (unsigned)(WS_NB + ((G * 4 + (KV ? 0 : 2) + a) * 2048 + rowbase + 16 * (2 * WN + m2) + l15) * 32 + g4 * 8), 0, SC1);
  }
  WAIT_L(0); BAR(); SCHED();
  flush_rows(t2, 4 * WN, 4 * WN + 4, c2);
  if constexpr (KV) {
    const int i4 = wm * 64 + lane, e = i4 >> 3, d0 = (i4 & 7) * 4;
    const int off = (((d0 >> 4) * 2 + (e >> 4)) * 64 + (e & 15) + 16 * ((d0 & 15) >> 2)) * 16;
    f32x4 s = {0.f, 0.f, 0.f, 0.f};
#pragma unroll
    for (int m = 0; m < 4; ++m) s += *(LAS f32x4*)(sc + (WN * 4 + m) * 4096 + off);
    __builtin_amdgcn_raw_buffer_store_b128(__builtin_bit_cast(u32x4, s), rws, (unsigned)(WS_KV + ((panel * 32 + 2 * hp + WN) * 1024 + i4 * 4) * 4), 0, SC1);
    if (wm == 0 && lane < 8) {
      const int db = lane >> 2, gq = lane & 3;
      f32x4 zs = {0.f, 0.f, 0.f, 0.f};
#pragma unroll
      for (int m = 0; m < 4; ++m) zs += *(LAS f32x4*)(sz + ((WN * 4 + m) * 2 + db) * 64 + gq * 16);
      __builtin_amdgcn_raw_buffer_store_b128(__builtin_bit_cast(u32x4, zs), rws, (unsigned)(WS_Z + ((panel * 32 + 2 * hp + WN) * 32 + 16 * db + 4 * gq) * 4), 0, SC1);
    }
  } else if constexpr (WN == 1) {
    LAS char* t1 = lds + K1_XS + wm * 8192;
#pragma unroll
    for (int mb = 0; mb < 4; ++mb) {
#pragma unroll
      for (int p = 0; p < 2; ++p) *(LAS u32x4*)(t1 + (16 * mb + l15) * 128 + (((4 * p + chunk_g) ^ (l15 & 7)) * 16)) = pack2(mish4(acc[mb][2 * p]), mish4(acc[mb][2 * p + 1]));
      flush_rows(t1, 2 * mb, 2 * mb + 2, c1);
    }
  }
}

DI void k2_compute(const Args& A, LAS char* lds, int tm, int tn, int role, int panel, bool isq, int hp, int wid) {
  int lane = fresh_lane();
  const int nt = role == 0 ? K2_NA + 16 : 56 - K2_NA, tile = tm * 8 + tn;
  const int wm = wid >> 2, wn = wid & 3;
  const int g4 = lane >> 4, l15 = lane & 15;
  BAR();
  bf16x8 pf[2][2];
  if (isq) {
#pragma unroll
    for (int mb = 0; mb < 2; ++mb)
#pragma unroll
      for (int h = 0; h < 2; ++h) {
        const int r = 32 * wid + 16 * mb + l15;
        const u32x4 raw = *(LAS u32x4*)(lds + K2_SCR + r * 128 + (((4 * h + g4) ^ (r & 7)) * 16));
        u32x4 w;
#pragma unroll
        for (int i = 0; i < 4; ++i) w[i] = cvt_pk(phi_f(bflo(raw[i])), phi_f(bfhi(raw[i])));
        pf[mb][h] = __builtin_bit_cast(bf16x8, w);
      }
  }
  const unsigned prow = (unsigned)panel * 256 + 32 * wid;
  const rsrc_t rws = mk_rsrc(A.ws);
  const unsigned ldsb = (unsigned)(size_t)lds;
  unsigned ax[2];
#pragma unroll
  for (int ks = 0; ks < 2; ++ks) ax[ks] = ldsb + (64 * wm + l15) * 128 + (((4 * ks + g4) ^ ((lane >> 1) & 7)) * 16);
  const int q4 = l15 >> 2, p4 = lane & 3;
  const int sr = ((q4 >> 1) & 1) | ((g4 & 1) << 1);
  unsigned aw[2];
#pragma unroll
  for (int nb = 0; nb < 2; ++nb) aw[nb] = ldsb + K2_WOFF + (wn >> 1) * 8192 + (8 * g4 + q4) * 128 + (((2 * (wn & 1) + nb) ^ sr) * 32) + p4 * 8;
  f32x4 acc[4][2];
#pragma unroll
  for (int i = 0; i < 4; ++i) acc[i][0] = acc[i][1] = (f32x4){0.f, 0.f, 0.f, 0.f};
  bf16x8 Af[2][4]; s16x4 Bl[2][2], Bh[2][2];
  auto rd = [&](auto KS, auto R, unsigned pa, const unsigned (&pw)[2]) {
    constexpr int ks = KS, r = R;
    if constexpr (r < 2) { if constexpr (r == 0) rdtr<ks * 4096>(Bl[ks][0], pw[0]); else rdtr<ks * 4096 + 512>(Bh[ks][0], pw[0]); }
    else if constexpr (r < 6) rd128<(r - 2) * 2048>(Af[ks][r - 2], pa);
    else { if constexpr (r == 6) rdtr<ks * 4096>(Bl[ks][1], pw[1]); else rdtr<ks * 4096 + 512>(Bh[ks][1], pw[1]); }
  };
  using I0 = std::integral_constant<int, 0>; using I1 = std::integral_constant<int, 1>;
  WAIT_L(0);
  BAR(); SCHED();
  static_for<8>([&](auto I) { rd(I0{}, I, ax[0], aw); });
  unsigned pa1 = ax[1], pwc[2] = {aw[0], aw[1]};
  auto body = [&](int i) {
    const unsigned so1 = ((i + 1) & 3) * K2_XS;
    unsigned pa0n = ax[0] + so1, pa1n = ax[1] + so1, pwn[2] = {aw[0] + so1, aw[1] + so1};
    pin(pa0n); pin(pa1n); pin(pwn[0]); pin(pwn[1]);
    WAIT_L(0); SCHED();
    static_for<8>([&](auto S) {
      constexpr int s = S, nb = s >> 2, mb = s & 3;
      acc[mb][nb] = MFMA16(CAT(Bl[0][nb], Bh[0][nb]), Af[0][mb], acc[mb][nb]);
      if constexpr (s < 4) { rd(I1{}, std::integral_constant<int, 2 * s>{}, pa1, pwc); rd(I1{}, std::integral_constant<int, 2 * s + 1>{}, pa1, pwc); }
      SCHED();
    });
    WAIT_L(0); SCHED();
    static_for<8>([&](auto S) {
      constexpr int s = S, nb = s >> 2, mb = s & 3;
      if constexpr (s == 2) { BAR(); SCHED(); }
      acc[mb][nb] = MFMA16(CAT(Bl[1][nb], Bh[1][nb]), Af[1][mb], acc[mb][nb]);
      if constexpr (s >= 2 && s < 6) { rd(I0{}, std::integral_constant<int, 2 * (s - 2)>{}, pa0n, pwn); rd(I0{}, std::integral_constant<int, 2 * (s - 2) + 1>{}, pa0n, pwn); }
      SCHED();
    });
    pa1 = pa1n; pwc[0] = pwn[0]; pwc[1] = pwn[1];
  };
#pragma unroll 1
  for (int i = 0; i <= K2_CHK; ++i) body(i);
  SCHED(); WAIT_L(0); SCHED();
  u32x4 part[8], zq; unsigned gv[2], gq[4];
  {
    const int lane = fresh_lane(), g4 = lane >> 4, l15 = lane & 15; (void)g4; (void)l15;
  if (isq) {
#pragma unroll
    for (int q = 0; q < 8; ++q) part[q] = __builtin_amdgcn_raw_buffer_load_b128(rws, (unsigned)(WS_KV + ((q * 32 + 2 * hp) * 1024 + (wid * 64 + lane) * 4) * 4), 0, SC1);
    zq = __builtin_amdgcn_raw_buffer_load_b128(rws, (unsigned)(WS_Z + ((lane >> 3) & 7) * 4096 + (2 * hp * 32 + (wid & 1) * 32 + (lane & 7) * 4) * 4), 0, SC1);
#pragma unroll
    for (int mb = 0; mb < 2; ++mb) gv[mb] = __builtin_amdgcn_raw_buffer_load_b32(rws, (unsigned)(WS_G + (prow + 16 * mb + l15) * 4), 0, SC1);
  }
#pragma unroll
  for (int mb = 0; mb < 4; ++mb) gq[mb] = __builtin_amdgcn_raw_buffer_load_b32(rws, (unsigned)(WS_G + (tm * 128 + 64 * wm + 16 * mb + l15) * 4), 0, SC1);
  }
  SCHED();
  body(K2_CHK + 1); body(K2_CHK + 2);
  LAS char* kvs = lds + K2_SCR;
  if (isq) {
    const int lane = fresh_lane(), g4 = lane >> 4, l15 = lane & 15; (void)g4; (void)l15;
    f32x4 ksum = {0.f, 0.f, 0.f, 0.f};
#pragma unroll
    for (int q = 0; q < 8; ++q) ksum += __builtin_bit_cast(f32x4, part[q]);
    f32x4 zsum = __builtin_bit_cast(f32x4, zq);
    if (wid < 2) {
#pragma unroll
      for (int o = 8; o <= 32; o <<= 1) { zsum[0] += __shfl_xor(zsum[0], o); zsum[1] += __shfl_xor(zsum[1], o); zsum[2] += __shfl_xor(zsum[2], o); zsum[3] += __shfl_xor(zsum[3], o); }
    }
    *(LAS u32x2*)(kvs + (wid * 64 + lane) * 8) = (u32x2){cvt_pk(ksum[0], ksum[1]), cvt_pk(ksum[2], ksum[3])};
    if (wid < 2 && lane < 8) *(LAS u32x2*)(kvs + 4096 + wid * 64 + lane * 8) = (u32x2){cvt_pk(zsum[0], zsum[1]), cvt_pk(zsum[2], zsum[3])};
    SCHED();
  }
  float omg[4];
#pragma unroll
  for (int mb = 0; mb < 4; ++mb) omg[mb] = 1.f - __uint_as_float(gq[mb]);
  body(K2_CHK + 3);
  if (isq) {
    const int lane = fresh_lane(), g4 = lane >> 4, l15 = lane & 15; (void)g4; (void)l15;
    const unsigned kb = (unsigned)(size_t)kvs;
    const int chunk_g = (g4 & 1) * 2 + (g4 >> 1);
#pragma unroll
    for (int h = 0; h < 2; ++h) {
      bf16x8 ka[2], za;
      rd128<0>(ka[0], kb + h * 2048 + l15 * 64 + g4 * 16); rd128<1024>(ka[1], kb + h * 2048 + l15 * 64 + g4 * 16); rd128<4096>(za, kb + h * 64 + g4 * 16);
      WAIT_L(0); SCHED();
#pragma unroll
      for (int mb = 0; mb < 2; ++mb) {
        const f32x4 zero = {0.f, 0.f, 0.f, 0.f};
        const f32x4 n0 = MFMA16(ka[0], pf[mb][h], zero), n1 = MFMA16(ka[1], pf[mb][h], zero), dn = MFMA16(za, pf[mb][h], zero);
        const float sc = __uint_as_float(gv[mb]) * __builtin_amdgcn_rcpf(dn[0] + 1e-6f);
        const f32x4 e = n0 * sc, o = n1 * sc;
        const unsigned e0 = cvt_pk(e[0], e[1]), e1 = cvt_pk(e[2], e[3]), o0 = cvt_pk(o[0], o[1]), o1 = cvt_pk(o[2], o[3]);
        const auto r0 = __builtin_amdgcn_permlane16_swap(e0, o0, false, false);
        const auto r1 = __builtin_amdgcn_permlane16_swap(e1, o1, false, false);
        const u32x4 w = {r0[0], r1[0], r0[1], r1[1]};
        __builtin_amdgcn_raw_buffer_store_b128(w, rws, (unsigned)(WS_XA + ((prow + 16 * mb + l15) * XA_LD + 3584 + 64 * hp + 32 * h + chunk_g * 8) * 2), 0, SC1);
      }
    }
    SCHED();
  }
  body(K2_CHK + 4); body(K2_CHK + 5);
  WAIT_V(0);
  WAIT_L(0); SCHED();
  const int n1 = role == 0 ? K2_NA : nt;
#pragma unroll 1
  for (int i = K2_CHK + 6; i < n1; ++i) body(i);
  SCHED(); WAIT_L(0); SCHED();
#pragma unroll
  for (int mb = 0; mb < 4; ++mb) { acc[mb][0] *= omg[mb]; acc[mb][1] *= omg[mb]; }
#pragma unroll 1
  for (int i = n1; i < nt; ++i) body(i);
  SCHED(); WAIT_L(0); SCHED();
  if (role == 1) {
    const int lane = fresh_lane(), g4 = lane >> 4, l15 = lane & 15; (void)g4; (void)l15;
    const unsigned poff = (unsigned)(WS_PART + (tile * 8 + wid) * 4096 + lane * 16);
#pragma unroll
    for (int mb = 0; mb < 4; ++mb) {
      const u32x4 w = {cvt_pk(acc[mb][0][0], acc[mb][0][1]), cvt_pk(acc[mb][0][2], acc[mb][0][3]), cvt_pk(acc[mb][1][0], acc[mb][1][1]), cvt_pk(acc[mb][1][2], acc[mb][1][3])};
      __builtin_amdgcn_raw_buffer_store_b128(w, rws, poff + mb * 1024, 0, SC1);
    }
    WAIT_V(0);
    BAR();
  } else {
    BAR();
    BAR();
    const int lane = fresh_lane(), g4 = lane >> 4, l15 = lane & 15; (void)g4; (void)l15;
    const int chunk_g = (g4 & 1) * 2 + (g4 >> 1);
    LAS char* pl = lds + K2_SCR + wid * 4096 + lane * 16;
#pragma unroll
    for (int mb = 0; mb < 4; ++mb) {
      const u32x4 pw_ = *(LAS u32x4*)(pl + mb * 1024);
      const f32x4 e = acc[mb][0] + (f32x4){bflo(pw_[0]), bfhi(pw_[0]), bflo(pw_[1]), bfhi(pw_[1])}, o = acc[mb][1] + (f32x4){bflo(pw_[2]), bfhi(pw_[2]), bflo(pw_[3]), bfhi(pw_[3])};
      const unsigned e0 = cvt_pk(e[0], e[1]), e1 = cvt_pk(e[2], e[3]), o0 = cvt_pk(o[0], o[1]), o1 = cvt_pk(o[2], o[3]);
      const auto r0 = __builtin_amdgcn_permlane16_swap(e0, o0, false, false);
      const auto r1 = __builtin_amdgcn_permlane16_swap(e1, o1, false, false);
      const u32x4 w = {r0[0], r1[0], r0[1], r1[1]};
      __builtin_amdgcn_raw_buffer_store_b128(w, mk_rsrc(A.out), (unsigned)(((tm * 128 + 64 * wm + 16 * mb + l15) * 1024 + tn * 128 + 32 * wn + chunk_g * 8) * 2), 0, SC1);
    }
  }
}

DI void loader(const Args& A, LAS char* lds, bool isx, int lane) {
  const int b = blockIdx.x, xcd = b & 7, idx = b >> 3, rq = xcd >> 1, ch = xcd & 1;
  const int panel = 2 * rq + (idx >> 4), c16 = idx & 15, hp = 8 * ch + (c16 >> 1), j32 = ch * 16 + c16;
  const bool kv = (c16 & 1) != 0;
  u64 ep; poll_issue(ep, A.ws + WS_EP);
  const int G = hp >> 1, a = hp & 1;
  const int c0 = (kv ? 1024 : 0) + 64 * hp;
  const int c1 = kv ? (2048 + 64 * hp) : (448 * G + 128 + 128 * a);
  const int c2 = kv ? (448 * G + 64 * a) : (448 * G + 128 + 128 * a + 64);
  const int c3 = 448 * G + 384 + (kv ? 0 : 32) + 16 * a;
  unsigned chx[2], chw[2];
#pragma unroll
  for (int par = 0; par < 2; ++par) {
    chx[par] = (unsigned)(((lane & 7) ^ ((4 * par + (lane >> 4)) & 7)) * 16);
    chw[par] = (unsigned)(((lane & 7) ^ ((((lane >> 4) & 1) | (par << 1)) << 1)) * 16);
  }
  unsigned tdummy = 0;
  u64 tag;
  {
    unsigned vA[4], vB[4], vN[2];
#pragma unroll
    for (int k = 0; k < 4; ++k) {
      vA[k] = (isx ? (unsigned)((8 * k + (lane >> 3)) * 2048) + chx[k & 1] : (unsigned)((8 * k + (lane >> 3)) * 6144) + chw[k & 1]) + 3072 - k * 1024;
      vB[k] = (unsigned)((8 * k + (lane >> 3)) * 7168) + chw[k & 1] + 3072 - k * 1024;
    }
#pragma unroll
    for (int n = 0; n < 2; ++n) { const int rho = 32 * n + (lane >> 1); vN[n] = (unsigned)((rho ^ (((rho >> 3) & 1) << 2)) * 7168 + (lane & 1) * 16) + 3072 - n * 1024; }
    const rsrc_t rx = mk_rsrc((const char*)A.x - 3072), rqk = mk_rsrc((const char*)A.wqkv - 3072), r1 = mk_rsrc((const char*)A.w1 - 3072);
    const unsigned xrow0 = (unsigned)panel * 256 * 2048;
    auto issueX = [&](int t, int xs) {
#pragma unroll
      for (int g = 0; g < 8; ++g)
        static_for<4>([&](auto K) { __builtin_amdgcn_raw_ptr_buffer_load_lds(rx, (LAS void*)(lds + xs * K1_XS + g * 4096), 16, vA[K], xrow0 + t * 128 + g * 65536, K * 1024, 0); });
    };
    auto issueW = [&](int t) {
      LAS char* dst = lds + K1_WOFF + (t & 1) * K1_WS;
#pragma unroll
      for (int g = 0; g < 2; ++g)
        static_for<4>([&](auto K) { __builtin_amdgcn_raw_ptr_buffer_load_lds(rqk, (LAS void*)(dst + g * 4096), 16, vA[K], (t * 64 + g * 32) * 6144 + c0 * 2, K * 1024, 0); });
      if (kv) {
#pragma unroll
        for (int g = 0; g < 2; ++g)
          static_for<4>([&](auto K) { __builtin_amdgcn_raw_ptr_buffer_load_lds(rqk, (LAS void*)(dst + 8192 + g * 4096), 16, vA[K], (t * 64 + g * 32) * 6144 + c1 * 2, K * 1024, 0); });
      } else {
#pragma unroll
        for (int g = 0; g < 2; ++g)
          static_for<4>([&](auto K) { __builtin_amdgcn_raw_ptr_buffer_load_lds(r1, (LAS void*)(dst + 8192 + g * 4096), 16, vB[K], (t * 64 + g * 32) * 7168 + c1 * 2, K * 1024, 0); });
      }
#pragma unroll
      for (int g = 0; g < 2; ++g)
        static_for<4>([&](auto K) { __builtin_amdgcn_raw_ptr_buffer_load_lds(r1, (LAS void*)(dst + 16384 + g * 4096), 16, vB[K], (t * 64 + g * 32) * 7168 + c2 * 2, K * 1024, 0); });
      static_for<2>([&](auto N) { __builtin_amdgcn_raw_ptr_buffer_load_lds(r1, (LAS void*)(dst + 24576), 16, vN[N], t * 64 * 7168 + c3 * 2, N * 1024, 0); });
    };
    auto touch3 = [&]() {
      const int w = b * 2 + (isx ? 0 : 1);
#pragma unroll
      for (int n = 0; n < 3; ++n) {
        const int k = n * 64 + lane, li = w * 144 + (k < 144 ? k : 143);
        touch(tdummy, li < 57344 ? (const char*)A.w2 + (size_t)li * 128 : (const char*)A.wo + (size_t)(li - 57344) * 128);
      }
    };
    if (isx) {
      issueX(0, 0); issueX(1, 1);
#pragma unroll
      for (int n = 0; n < 4; ++n) touch(tdummy, (const char*)A.x + (size_t)(panel * 256 + 16 * c16 + 4 * n + (lane >> 4)) * 2048 + (lane & 15) * 128);
      auto wtouch = [&](int t0) {
        const int li = b * 52 + (lane < 52 ? lane : 51);
        int row = 64 * t0 + li / 104; row = row < 1024 ? row : 1023;
        const int c = li % 104;
        touch(tdummy, c < 48 ? (const char*)A.wqkv + (size_t)row * 6144 + c * 128 : (const char*)A.w1 + (size_t)row * 7168 + (c - 48) * 128);
      };
      auto wtouch1 = [&](int t0) {
        const int li = b * 26 + (lane < 26 ? lane : 25);
        const int row = 64 * (t0 < 16 ? t0 : 15) + li / 104, c = li % 104;
        touch(tdummy, c < 48 ? (const char*)A.wqkv + (size_t)row * 6144 + c * 128 : (const char*)A.w1 + (size_t)row * 7168 + (c - 48) * 128);
      };
      constexpr int WD1 = 4;
      wtouch(2);
      WAIT_V(37); pin(ep); tag = ep + 1;
      BAR();
      if (lane == 0) __hip_atomic_store((gu64*)(A.ws + WS_START) + b, tag, RLX_AGENT);
      issueX(2, 2);
#pragma unroll
      for (int t = 4; t < WD1 + 1; ++t) wtouch1(t);
      int xs = 0;
#pragma unroll 1
      for (int t = 0; t < 13; ++t) {
        WAIT_V(33); BAR(); issueX(t + 3, xs); xs = xs == 2 ? 0 : xs + 1;
        wtouch1(t + 1 + WD1);
      }
      touch3();
      WAIT_V(35); BAR();
      WAIT_V(3); BAR();
      u32x4 gx[8][2], gg[2];
#pragma unroll
      for (int r = 0; r < 8; ++r) { const char* xr = (const char*)A.x + (size_t)(panel * 256 + j32 * 8 + r) * 2048 + lane * 32; gload4(gx[r][0], xr); gload4(gx[r][1], xr + 16); }
      gload4(gg[0], (const char*)A.gw + lane * 32); gload4(gg[1], (const char*)A.gw + lane * 32 + 16);
      BAR();
      BAR();
      WAIT_V(0);
      {
        pin(gg[0]); pin(gg[1]);
        float s[8];
#pragma unroll
        for (int r = 0; r < 8; ++r) {
          pin(gx[r][0]); pin(gx[r][1]);
          s[r] = 0.f;
#pragma unroll
          for (int h = 0; h < 2; ++h)
#pragma unroll
            for (int i = 0; i < 4; ++i) { const unsigned ux = gx[r][h][i], ug = gg[h][i]; s[r] = __builtin_amdgcn_fdot2_f32_bf16(__builtin_bit_cast(bf16x2_t, ux), __builtin_bit_cast(bf16x2_t, ug), s[r], false); }
        }
#pragma unroll
        for (int o = 32; o >= 1; o >>= 1)
#pragma unroll
          for (int r = 0; r < 8; ++r) s[r] += __shfl_xor(s[r], o);
        float mine = s[0];
#pragma unroll
        for (int r = 1; r < 8; ++r) mine = lane == r ? s[r] : mine;
        const float gval = __builtin_amdgcn_rcpf(1.f + __builtin_amdgcn_exp2f(-LOG2E * mine));
        if (lane < 8) __hip_atomic_store((__attribute__((address_space(1))) unsigned*)(A.ws + WS_G) + panel * 256 + j32 * 8 + lane, __float_as_uint(gval), RLX_AGENT);
      }
    } else {
      issueW(0); issueW(1);
      WAIT_V(26); pin(ep); tag = ep + 1;
      BAR();
#pragma unroll 1
      for (int t = 0; t < 14; ++t) { WAIT_V(0); BAR(); issueW(t + 2); }
      touch3();
      WAIT_V(3); BAR();
      BAR();
    }
  }
  const int role = (idx >> 1) & 1, tm = 4 * rq + (idx >> 3), tn = 4 * ch + 2 * ((idx >> 2) & 1) + (idx & 1), tile = tm * 8 + tn;
  const int nt = role == 0 ? K2_NA + 16 : 56 - K2_NA;
  unsigned voff[4];
#pragma unroll
  for (int k = 0; k < 4; ++k)
    voff[k] = (isx ? (unsigned)((8 * k + (lane >> 3)) * (XA_LD * 2) + chx[k & 1]) : (unsigned)((8 * k + (lane >> 3)) * 2048 + chw[k & 1])) + 3072 - k * 1024;
  const unsigned gstep = isx ? 32 * XA_LD * 2 : 32 * 2048;
  auto issue = [&](rsrc_t r, unsigned so, int slot) {
    LAS char* st = lds + (isx ? 0 : K2_WOFF) + slot * K2_XS;
#pragma unroll
    for (int g = 0; g < 4; ++g) {
      const unsigned sg = isx ? so + g * gstep : so + (g & 1) * gstep + (g >> 1) * 128;
      static_for<4>([&](auto K) { __builtin_amdgcn_raw_ptr_buffer_load_lds(r, (LAS void*)(st + g * 4096), 16, voff[K], sg, K * 1024, SC1); });
    }
  };
  unsigned voffN[4];
#pragma unroll
  for (int k = 0; k < 4; ++k) { const unsigned c = chx[k & 1] >> 4; voffN[k] = (c >> 1) * 65536 + (8 * k + (lane >> 3)) * 32 + (c & 1) * 16 + 3072 - k * 1024; }
  const rsrc_t rn = mk_rsrc((const char*)(A.ws + WS_NB) - 3072);
  auto issueN = [&](int grp, int slot) {
    LAS char* st = lds + slot * K2_XS;
#pragma unroll
    for (int g = 0; g < 4; ++g)
      static_for<4>([&](auto K) { __builtin_amdgcn_raw_ptr_buffer_load_lds(rn, (LAS void*)(st + g * 4096), 16, voffN[K], (unsigned)grp * 262144 + (unsigned)(tm * 128 + g * 32) * 32, K * 1024, SC1); });
  };
  const rsrc_t r0 = mk_rsrc(isx ? (const char*)(A.ws + WS_XA) - 3072 : (const char*)A.w2 - 3072);
  const rsrc_t r1 = mk_rsrc(isx ? (const char*)(A.ws + WS_XA) - 3072 : (const char*)A.wo - 3072);
  const unsigned tstep = isx ? 128u : 64u * 2048u;
  const unsigned sbase = isx ? (unsigned)tm * 128 * (XA_LD * 2) : (unsigned)tn * 256;
  const unsigned s0 = sbase + (role == 0 ? 0 : K2_NA) * tstep;
  const unsigned s1 = isx ? sbase + 56 * tstep : sbase;
  const rsrc_t rp = mk_rsrc(A.ws + WS_PART);
  auto issue_partial = [&]() {
    LAS char* dst = lds + K2_SCR + (isx ? 0 : 16384);
#pragma unroll
    for (int g = 0; g < 4; ++g)
      static_for<4>([&](auto K) { __builtin_amdgcn_raw_ptr_buffer_load_lds(rp, (LAS void*)(dst + g * 4096), 16, lane * 16, (unsigned)tile * 32768 + (isx ? 0 : 16384) + g * 4096, K * 1024, SC1); });
  };
  auto issueA = [&](int pos) {
    const int kt = (role == 0 ? 0 : K2_NA) + pos, grp = kt / 7;
    if (isx && kt - 7 * grp == 6) issueN(grp, pos & 3); else issue(r0, s0 + pos * tstep, pos & 3);
  };
  if (!isx) { issueA(0); issueA(1); issueA(2); issueA(3); BAR(); }
  if (isx) WAIT_V(0);
  BAR();
  unsigned ready = 0;
  auto need = [&](int pos) {
    const int grp = ((role == 0 ? 0 : K2_NA) + pos) / 7;
    for (unsigned spins = 0; ((ready >> (4 * grp)) & 15u) != 15u && spins < (1u << 20); ++spins) {
      const u64 v = __hip_atomic_load((const gu64*)(A.ws + WS_DONE1) + panel * 32 + (lane & 31), RLX_AGENT);
      ready = (unsigned)__ballot(v == tag);
    }
  };
  if (isx) {
    if (lane == 0) __hip_atomic_store((gu64*)(A.ws + WS_DONE1) + panel * 32 + j32, tag, RLX_AGENT);
    need(0); issueA(0); need(1); issueA(1); need(2); issueA(2); WAIT_V(32);
  } else WAIT_V(48);
  BAR();
  if (isx) { need(3); issueA(3); }
  auto run = [&](int i0, int i1, rsrc_t r, unsigned so) {
#pragma unroll 1
    for (int i = i0; i < i1; ++i, so += tstep) { WAIT_V(32); BAR(); issue(r, so, i & 3); }
  };
  auto runA = [&](int i0, int i1) {
#pragma unroll 1
    for (int i = i0; i < i1; ++i) { WAIT_V(32); BAR(); issueA(i + 4); }
  };
  u64 pv = 0;
#pragma unroll 1
  for (int i = 0; i < K2_CHK; ++i) { WAIT_V(32); BAR(); if (isx) need(i + 4); issueA(i + 4); }
  if (isx) {
    const int slot = lane < 32 ? panel * 32 + lane : ((lane < 40 && !kv) ? (lane - 32) * 32 + (j32 | 1) : panel * 32);
    wait_all(A.ws + WS_DONE1 + (size_t)slot * 8, tag);
  }
  runA(K2_CHK, K2_CHK + 7);
  if (isx && !kv && lane == 0) __hip_atomic_store((gu64*)(A.ws + WS_ODONE) + panel * 16 + hp, tag, RLX_AGENT);
  if (role == 0) {
    runA(K2_CHK + 7, K2_NA - 8);
    const char* od = A.ws + WS_ODONE + (size_t)(panel * 16 + (lane & 15)) * 8;
    if (isx) poll_issue(pv, (const void*)od);
    runA(K2_NA - 8, K2_NA - 4);
    if (isx) { WAIT_V(32); pin(pv); if (!__all(pv == tag)) wait_all((const void*)od, tag); }
    run(K2_NA - 4, nt - 4, r1, s1);
  } else runA(K2_CHK + 7, nt - 4);
  const char* pd = A.ws + WS_PDONE + (size_t)tile * 128;
  poll_issue(pv, (const void*)pd);
  WAIT_V(33); BAR();
  WAIT_V(17); BAR();
  WAIT_V(0);
  bool early = false;
  if (role == 0) { pin(pv); early = __all(pv == tag); }
  BAR();
  if (early) issue_partial();
  BAR();
  if (role == 0) {
    if (!early) wait_all((const void*)pd, tag);
    BAR();
    if (!early) issue_partial();
    WAIT_V(0);
    BAR();
  } else {
    BAR();
    if (isx && lane == 0) __hip_atomic_store((gu64*)(A.ws + WS_PDONE + (size_t)tile * 128), tag, RLX_AGENT);
  }
  if (b == 0 && isx) {
#pragma unroll 1
    for (int k = 0; k < 4; ++k) wait_all(A.ws + WS_START + (size_t)(k * 64 + lane) * 8, tag);
    if (lane == 0) __hip_atomic_store((gu64*)(A.ws + WS_EP), tag, RLX_AGENT);
  }
  WAIT_V(0); keep(tdummy);
}

__global__ __launch_bounds__(640, 3) void fused_kernel(Args A) {
  extern __shared__ __attribute__((aligned(16))) char smem[];
  LAS char* lds = (LAS char*)smem;
  const int b = blockIdx.x, xcd = b & 7, idx = b >> 3, rq = xcd >> 1, ch = xcd & 1;
  const int panel = 2 * rq + (idx >> 4), c16 = idx & 15, hp = 8 * ch + (c16 >> 1);
  const int wid = __builtin_amdgcn_readfirstlane(threadIdx.x >> 6);
  if (wid >= 8) { loader(A, lds, wid == 8, fresh_lane()); return; }
  const int lane = fresh_lane();
  if (c16 & 1) {
    if (wid < 4) k1_body<true, 0>(A, lds, panel, hp, wid, lane); else k1_body<true, 1>(A, lds, panel, hp, wid - 4, lane);
  } else {
    if (wid < 4) k1_body<false, 0>(A, lds, panel, hp, wid, lane); else k1_body<false, 1>(A, lds, panel, hp, wid - 4, lane);
  }
  k2_compute(A, lds, 4 * rq + (idx >> 3), 4 * ch + 2 * ((idx >> 2) & 1) + (idx & 1), (idx >> 1) & 1, panel, (c16 & 1) == 0, hp, wid);
}

extern "C" void kernel_launch(void* const* d_in, const int* in_sizes, int n_in, void* d_out, int out_size, void* d_ws, size_t ws_size, hipStream_t stream) {
  Args a; a.x = (const u16*)d_in[0]; a.wqkv = (const u16*)d_in[1]; a.w1 = (const u16*)d_in[4]; a.b1 = (const u16*)d_in[5]; a.gw = (const u16*)d_in[7]; a.ws = (char*)d_ws;
  a.w2 = (const u16*)d_in[6]; a.wo = (const u16*)d_in[2]; a.out = (u16*)d_out;
  (void)hipFuncSetAttribute((const void*)fused_kernel, hipFuncAttributeMaxDynamicSharedMemorySize, LDS_BYTES);
  fused_kernel<<<256, 640, LDS_BYTES, stream>>>(a);
}
```
